```python
import torch

def _softmax(x, axis=-1):
    return torch.softmax(x, dim=axis)
def _gelu(x):
    return torch.nn.functional.gelu(x, approximate="tanh")
def _silu(x):
    return torch.nn.functional.silu(x)
def _sigmoid(x):
    return torch.sigmoid(x)
def _layernorm(x, axis=-1, eps=1e-5):
    m = x.mean(dim=axis, keepdim=True)
    v = x.var(dim=axis, keepdim=True, unbiased=False)
    return (x - m) / torch.sqrt(v + eps)
def _scatter_add(tgt, idx, src, axis=0):
    return tgt.clone().index_add_(axis, idx, src)
def _segment_sum(src, seg, n):
    out = torch.zeros((n,) + src.shape[1:], dtype=src.dtype)
    return out.index_add_(0, seg, src)
def _pad(x, pw):
    flat = tuple(v for pair in reversed(pw) for v in pair)
    return torch.nn.functional.pad(x, flat)

kernel_name = 'sequential_preLN__gqa_block_cached__mixer_token_mlp__decode'

def setup_inputs():

    _w = (lambda a: (lambda t: t.to(torch.bfloat16) if t.is_floating_point() else t)(torch.as_tensor(a)))
    def _A_setup():
        B, Sq, D = 2, 1, 576
        Hq, Hkv, Sk, Dh = 32, 16, 32768, 128
        assert Hq % Hkv == 0, (Hq, Hkv)
        return {
            "x": _w((lambda *s: torch.randn(*s, dtype=torch.bfloat16) * 0.05)(B, Sq, D)),
            "k_cache": _w((lambda *s: torch.randn(*s, dtype=torch.bfloat16) * 3.2)(B, Hkv, Sk, Dh)),
            "v_cache": _w((lambda *s: torch.randn(*s, dtype=torch.bfloat16) * 0.8)(B, Hkv, Sk, Dh)),
            "wq": _w((lambda *s: torch.randn(*s, dtype=torch.bfloat16) * 3.2)(D, Hq * Dh) * (D ** -0.5)),
            "wo": _w((lambda *s: torch.randn(*s, dtype=torch.bfloat16) * 0.8)(Hq * Dh, D) * ((Hq * Dh) ** -0.5)),
        }
    _A_in = _A_setup()

    def _B_setup():
        B, S, D, F = 2, 1, 576, 4608
        return {
            "x": _w((lambda *s: torch.randn(*s, dtype=torch.bfloat16) * 0.05)(B, S, D)),
            "w1": _w((lambda *s: torch.randn(*s, dtype=torch.bfloat16) * 0.8)(S, F) * (S ** -0.5)),
            "b1": _w((lambda *s: torch.randn(*s, dtype=torch.bfloat16) * 0.8)(F) * 0.02),
            "w2": _w((lambda *s: torch.randn(*s, dtype=torch.bfloat16) * 0.8)(F, S) * (F ** -0.5)),
            "b2": _w((lambda *s: torch.randn(*s, dtype=torch.bfloat16) * 0.8)(S) * 0.02),
        }
    _B_in = _B_setup()

    return {
        "A_x": _A_in["x"],
        "A_k_cache": _A_in["k_cache"],
        "A_v_cache": _A_in["v_cache"],
        "A_wq": _A_in["wq"],
        "A_wo": _A_in["wo"],
        "B_x": _B_in["x"],
        "B_w1": _B_in["w1"],
        "B_b1": _B_in["b1"],
        "B_w2": _B_in["w2"],
        "B_b2": _B_in["b2"],
    }

def reference(A_x, A_k_cache, A_v_cache, A_wq, A_wo, B_x, B_w1, B_b1, B_w2, B_b2):
    def _A_ref(x, k_cache, v_cache, wq, wo):
        B, Sq, D = x.shape
        Hq, Dh = 32, 128
        Hkv = k_cache.shape[1]
        rep = Hq // Hkv
        q = (lambda x, *d: x.permute(*d))((x @ wq).reshape(B, Sq, Hq, Dh), 0, 2, 1, 3)
        k = (lambda x, n, axis: torch.repeat_interleave(x, n, dim=axis))(k_cache, rep, axis=1)
        v = (lambda x, n, axis: torch.repeat_interleave(x, n, dim=axis))(v_cache, rep, axis=1)
        scores = torch.einsum("bhqd,bhkd->bhqk", q, k) * (Dh ** -0.5)
        p = _softmax(scores, axis=-1)
        o = torch.einsum("bhqk,bhkd->bhqd", p, v)
        return (lambda x, *d: x.permute(*d))(o, 0, 2, 1, 3).reshape(B, Sq, Hq * Dh) @ wo

    def _B_ref(x, w1, b1, w2, b2):
        B, S, D = x.shape
        h = _layernorm(x, axis=-1)
        u = torch.einsum("bsd,sf->bdf", h, w1) + b1
        u = _gelu(u)
        y = torch.einsum("bdf,fs->bds", u, w2) + b2
        return (lambda x, *d: x.permute(*d))(y, 0, 2, 1)

    x = A_x
    h = x + _A_ref(_layernorm(x, axis=-1), A_k_cache, A_v_cache, A_wq, A_wo)
    h2 = h + _B_ref(_layernorm(h, axis=-1), B_w1, B_b1, B_w2, B_b2)
    return h2
```

```cpp
#include <hip/hip_runtime.h>
#include <hip/hip_bf16.h>
#include <stdint.h>

typedef short s4 __attribute__((ext_vector_type(4)));
typedef __bf16 bf2 __attribute__((ext_vector_type(2)));
typedef __bf16 bf4 __attribute__((ext_vector_type(4)));
typedef __bf16 bf8 __attribute__((ext_vector_type(8)));
typedef float f2 __attribute__((ext_vector_type(2)));
typedef float f4 __attribute__((ext_vector_type(4)));
typedef unsigned u2 __attribute__((ext_vector_type(2)));
typedef unsigned u4 __attribute__((ext_vector_type(4)));
#define LAS __attribute__((address_space(3)))
#define GAS __attribute__((address_space(1)))

constexpr int DM = 576, HQ = 32, HKV = 16, SK = 32768, DH = 128, FF = 4608;
constexpr float EPS = 1e-5f;
constexpr float CEXP = 0.08838834764831845f * 1.4426950408889634f;

__device__ __forceinline__ float bf2f(unsigned short v) { return __uint_as_float((unsigned)v << 16); }
__device__ __forceinline__ float rbf(float x) { return (float)(__bf16)x; }
__device__ __forceinline__ float wave_sum(float v) {
#pragma unroll
  for (int m = 32; m >= 1; m >>= 1) v += __shfl_xor(v, m);
  return v;
}

__global__ void __launch_bounds__(256) k_q(const unsigned short* __restrict__ x, const unsigned short* __restrict__ wq, unsigned short* __restrict__ q) {
  __shared__ float xl[2][DM];
  __shared__ float red[8][2][32];
  const int tid = threadIdx.x, lane = tid & 63, w = tid >> 6;
  if (w < 2) {
    float v[9], s = 0.f;
    for (int i = 0; i < 9; ++i) { v[i] = bf2f(x[w * DM + lane + 64 * i]); s += v[i]; }
    const float mean = wave_sum(s) / DM;
    float ss = 0.f;
    for (int i = 0; i < 9; ++i) { v[i] -= mean; ss += v[i] * v[i]; }
    const float rstd = rsqrtf(wave_sum(ss) / DM + EPS);
    for (int i = 0; i < 9; ++i) xl[w][lane + 64 * i] = rbf(v[i] * rstd);
  }
  __syncthreads();
  const int col = blockIdx.x * 32 + (tid & 31), kg = tid >> 5;
  float a0 = 0.f, a1 = 0.f;
#pragma unroll 8
  for (int k = kg * 72; k < kg * 72 + 72; ++k) { const float wv = bf2f(wq[(size_t)k * 4096 + col]); a0 += xl[0][k] * wv; a1 += xl[1][k] * wv; }
  red[kg][0][tid & 31] = a0; red[kg][1][tid & 31] = a1;
  __syncthreads();
  if (tid < 64) { float s = 0.f; for (int g = 0; g < 8; ++g) s += red[g][tid >> 5][tid & 31];
    __bf16 r = (__bf16)s; q[(tid >> 5) * 4096 + blockIdx.x * 32 + (tid & 31)] = __builtin_bit_cast(unsigned short, r); }
}

constexpr int NSLOT = 4, SLOTB = 8192, NT = 64;
template <int D>
__global__ void __launch_bounds__(256, 1) k_attn(const char* __restrict__ K, const char* __restrict__ V, const unsigned short* __restrict__ q, float* __restrict__ part) {
  extern __shared__ __attribute__((aligned(16))) unsigned char lds[];
  LAS unsigned char* L = (LAS unsigned char*)lds;
  const int tid = threadIdx.x, lane = tid & 63, wave = __builtin_amdgcn_readfirstlane(tid >> 6);
  const int wg = blockIdx.x, grp = wg >> 3;
  const int g = lane >> 4, c = lane & 15;
  const char* kb = K + (size_t)wg * (4096 * 256) + wave * 4096;
  const char* vb = V + (size_t)wg * (4096 * 256) + wave * 4096;
  unsigned vk[4], vv[2];
#pragma unroll
  for (int j = 0; j < 4; ++j) vk[j] = 256 * g + 16 * ((c ^ g) ^ (4 * j));
#pragma unroll
  for (int j = 0; j < 2; ++j) vv[j] = 256 * g + 16 * (c ^ (2 * g) ^ (8 * j));
  const unsigned ldsw = (unsigned)(uintptr_t)L + wave * (NSLOT * SLOTB);
#define ISSUE(tile, slot) do { const char* kp_ = kb + (size_t)(tile) * 16384; const char* vp_ = vb + (size_t)(tile) * 16384; \
    asm volatile("s_waitcnt lgkmcnt(0)\n\ts_mov_b32 m0, %8\n\ts_nop 0\n\t" \
      "global_load_lds_dwordx4 %0, %6 nt\n\tglobal_load_lds_dwordx4 %1, %6 offset:1024 nt\n\tglobal_load_lds_dwordx4 %2, %6 offset:2048 nt\n\tglobal_load_lds_dwordx4 %3, %6 offset:3072 nt\n\t" \
      "s_mov_b32 m0, %9\n\ts_nop 0\n\t" \
      "global_load_lds_dwordx4 %4, %7 nt\n\tglobal_load_lds_dwordx4 %5, %7 offset:1024 nt\n\tglobal_load_lds_dwordx4 %4, %7 offset:2048 nt\n\tglobal_load_lds_dwordx4 %5, %7 offset:3072 nt" \
      :: "v"(vk[0]), "v"(vk[1]), "v"(vk[2]), "v"(vk[3]), "v"(vv[0]), "v"(vv[1]), "s"(kp_), "s"(vp_), "s"(ldsw + (slot) * SLOTB), "s"(ldsw + (slot) * SLOTB + 4096) : "memory"); } while (0)
#pragma unroll
  for (int i = 0; i < D; ++i) ISSUE(i, i);
  bf8 qf[4];
  {
    const unsigned short* qp = q + (grp >> 4) * 4096 + ((grp & 15) * 2 + (c & 1)) * 128 + 8 * g;
    u4 t0, t1, t2, t3;
    asm volatile("global_load_dwordx4 %0, %4, off\n\tglobal_load_dwordx4 %1, %4, off offset:64\n\tglobal_load_dwordx4 %2, %4, off offset:128\n\tglobal_load_dwordx4 %3, %4, off offset:192\n\ts_waitcnt vmcnt(0)"
      : "=&v"(t0), "=&v"(t1), "=&v"(t2), "=&v"(t3) : "v"(qp) : "memory");
    qf[0] = __builtin_bit_cast(bf8, t0); qf[1] = __builtin_bit_cast(bf8, t1); qf[2] = __builtin_bit_cast(bf8, t2); qf[3] = __builtin_bit_cast(bf8, t3);
  }
  LAS unsigned char* ka[4]; LAS unsigned char* va[8];
#pragma unroll
  for (int s = 0; s < 4; ++s) ka[s] = L + wave * (NSLOT * SLOTB) + c * 256 + 16 * ((g ^ c) ^ (4 * s));
  { const int row = 4 * g + ((lane >> 2) & 3), p = lane & 3;
#pragma unroll
    for (int nb = 0; nb < 8; ++nb) va[nb] = L + wave * (NSLOT * SLOTB) + 4096 + row * 256 + 32 * (nb ^ (row & 7)) + 8 * p; }
  float m = -INFINITY, mc = -INFINITY, l = 0.f;
  f4 acc[8];
#pragma unroll
  for (int nb = 0; nb < 8; ++nb) acc[nb] = f4{0.f, 0.f, 0.f, 0.f};
  auto tile = [&](const int slot, auto issue) {
    bf8 kf[4]; bf4 vf[8];
#pragma unroll
    for (int s = 0; s < 4; ++s) kf[s] = *(LAS bf8*)(ka[s] + slot * SLOTB);
#pragma unroll
    for (int nb = 0; nb < 8; ++nb) vf[nb] = __builtin_amdgcn_ds_read_tr16_b64_v4bf16((LAS bf4*)(va[nb] + slot * SLOTB));
    issue();
    f4 sc = {0.f, 0.f, 0.f, 0.f};
#pragma unroll
    for (int s = 0; s < 4; ++s) sc = __builtin_amdgcn_mfma_f32_16x16x32_bf16(kf[s], qf[s], sc, 0, 0, 0);
    float t = fmaxf(fmaxf(sc[0], sc[1]), fmaxf(sc[2], sc[3]));
    if (__any(t > m)) {
      t = fmaxf(t, __shfl_xor(t, 16)); t = fmaxf(t, __shfl_xor(t, 32));
      const float mn = fmaxf(m, t);
      const float alpha = __builtin_amdgcn_exp2f((m - mn) * CEXP);
      m = mn; mc = mn * CEXP; l *= alpha;
      const float a0 = __builtin_bit_cast(float, __builtin_amdgcn_readlane(__builtin_bit_cast(int, alpha), 0));
      const float a1 = __builtin_bit_cast(float, __builtin_amdgcn_readlane(__builtin_bit_cast(int, alpha), 1));
#pragma unroll
      for (int nb = 0; nb < 8; ++nb) { acc[nb][0] *= a0; acc[nb][1] *= a1; }
    }
    const float p0 = __builtin_amdgcn_exp2f(fmaf(sc[0], CEXP, -mc)), p1 = __builtin_amdgcn_exp2f(fmaf(sc[1], CEXP, -mc));
    const float p2 = __builtin_amdgcn_exp2f(fmaf(sc[2], CEXP, -mc)), p3 = __builtin_amdgcn_exp2f(fmaf(sc[3], CEXP, -mc));
    l += (p0 + p1) + (p2 + p3);
    const bf2 pa = {(__bf16)p0, (__bf16)p1}, pb = {(__bf16)p2, (__bf16)p3};
    const u2 pk = {__builtin_bit_cast(unsigned, pa), __builtin_bit_cast(unsigned, pb)};
#pragma unroll
    for (int nb = 0; nb < 8; ++nb) acc[nb] = __builtin_amdgcn_mfma_f32_16x16x16bf16_1k(__builtin_bit_cast(s4, pk), __builtin_bit_cast(s4, vf[nb]), acc[nb], 0, 0, 0);
  };
#define WAITV(n) asm volatile("s_waitcnt vmcnt(%0)" :: "i"(n) : "memory")
  for (int i0 = 0; i0 < NT - NSLOT; i0 += NSLOT) {
#pragma unroll
    for (int u = 0; u < NSLOT; ++u) {
      WAITV(8 * (D - 1));
      tile(u, [&]() { ISSUE(i0 + u + D, (u + D) % NSLOT); });
    }
  }
#pragma unroll
  for (int u = 0; u < NSLOT; ++u) {
    constexpr int i0 = NT - NSLOT;
    const int rem = NSLOT - 1 - u;
    if (rem >= D - 1) WAITV(8 * (D - 1)); else if (rem == 2) WAITV(16); else if (rem == 1) WAITV(8); else WAITV(0);
    if (u + D < NSLOT) tile(u, [&]() { ISSUE(i0 + u + D, (u + D) % NSLOT); }); else tile(u, [&]() {});
  }
  l += __shfl_xor(l, 16); l += __shfl_xor(l, 32);
  __syncthreads();
  LAS float* mg = (LAS float*)L;
  if (lane < 16) {
#pragma unroll
    for (int nb = 0; nb < 8; ++nb) { mg[(wave * 2 + 0) * 132 + 16 * nb + c] = acc[nb][0]; mg[(wave * 2 + 1) * 132 + 16 * nb + c] = acc[nb][1]; }
    if (lane < 2) { mg[(wave * 2 + lane) * 132 + 128] = mc; mg[(wave * 2 + lane) * 132 + 129] = l; }
  }
  __syncthreads();
  const int h = tid >> 7, d = tid & 127;
  float M = -INFINITY;
#pragma unroll
  for (int w = 0; w < 4; ++w) M = fmaxf(M, mg[(w * 2 + h) * 132 + 128]);
  float o = 0.f, Ls = 0.f;
#pragma unroll
  for (int w = 0; w < 4; ++w) { const float f = __builtin_amdgcn_exp2f(mg[(w * 2 + h) * 132 + 128] - M); o += f * mg[(w * 2 + h) * 132 + d]; Ls += f * mg[(w * 2 + h) * 132 + 129]; }
  float* pp = part + ((size_t)wg * 2 + h) * 132;
  pp[d] = o;
  if (d == 0) { pp[128] = M; pp[129] = Ls; }
}

__global__ void __launch_bounds__(256) k_proj(const float* __restrict__ part, const unsigned short* __restrict__ wo, float* __restrict__ r) {
  __shared__ float o[2][256];
  __shared__ float red[4][2][64];
  const int tid = threadIdx.x, kvh = blockIdx.x / 9, cb = blockIdx.x % 9;
  const int h = tid >> 7, d = tid & 127;
  for (int b = 0; b < 2; ++b) {
    const float* pp = part + ((size_t)(b * 16 + kvh) * 8 * 2 + h) * 132;
    float M = -INFINITY;
    for (int s = 0; s < 8; ++s) M = fmaxf(M, pp[s * 264 + 128]);
    float a = 0.f, Ls = 0.f;
    for (int s = 0; s < 8; ++s) { const float f = __builtin_amdgcn_exp2f(pp[s * 264 + 128] - M); a += f * pp[s * 264 + d]; Ls += f * pp[s * 264 + 129]; }
    o[b][tid] = rbf(a / Ls);
  }
  __syncthreads();
  const int col = cb * 64 + (tid & 63), kg = tid >> 6;
  float a0 = 0.f, a1 = 0.f;
#pragma unroll 8
  for (int k = kg * 64; k < kg * 64 + 64; ++k) { const float wv = bf2f(wo[(size_t)(kvh * 256 + k) * DM + col]); a0 += o[0][k] * wv; a1 += o[1][k] * wv; }
  red[kg][0][tid & 63] = a0; red[kg][1][tid & 63] = a1;
  __syncthreads();
  if (tid < 128) { const int b = tid >> 6; r[(kvh * 2 + b) * DM + cb * 64 + (tid & 63)] = red[0][b][tid & 63] + red[1][b][tid & 63] + red[2][b][tid & 63] + red[3][b][tid & 63]; }
}

__global__ void __launch_bounds__(512) k_mlp(const float* __restrict__ r, const unsigned short* __restrict__ x, const unsigned short* __restrict__ w1, const unsigned short* __restrict__ b1,
                                             const unsigned short* __restrict__ w2, const unsigned short* __restrict__ b2, unsigned short* __restrict__ out) {
  __shared__ float hs[DM], zs[DM];
  __shared__ float red[8][5];
  const int tid = threadIdx.x, lane = tid & 63, w = tid >> 6, b = blockIdx.x >> 7, j = blockIdx.x & 127;
  const int p0 = 9 * (j >> 1) + (j & 1) * 5, np = (j & 1) ? 4 : 5;
  for (int d = tid; d < DM; d += 512) { float s = 0.f; for (int k = 0; k < 16; ++k) s += r[(k * 2 + b) * DM + d]; hs[d] = rbf(bf2f(x[b * DM + d]) + rbf(s)); }
  __syncthreads();
  if (w == 0) {
    float v[9], s = 0.f;
    for (int i = 0; i < 9; ++i) { v[i] = hs[lane + 64 * i]; s += v[i]; }
    for (int rep = 0; rep < 2; ++rep) {
      const float mean = wave_sum(s) / DM;
      float ss = 0.f;
      for (int i = 0; i < 9; ++i) { v[i] -= mean; ss += v[i] * v[i]; }
      const float rstd = rsqrtf(wave_sum(ss) / DM + EPS);
      s = 0.f;
      for (int i = 0; i < 9; ++i) { v[i] = v[i] * rstd; s += v[i]; }
    }
    for (int i = 0; i < 9; ++i) zs[lane + 64 * i] = v[i];
  }
  __syncthreads();
  float acc[5] = {0.f, 0.f, 0.f, 0.f, 0.f};
  for (int i = 0; i < 9; ++i) {
    const int f = tid + 512 * i;
    const float a = bf2f(w1[f]), bb = bf2f(b1[f]), cc = bf2f(w2[f]);
#pragma unroll
    for (int p = 0; p < 5; ++p) {
      const float z = zs[min(p0 + p, DM - 1)];
      const float u = fmaf(z, a, bb);
      const float t = u * fmaf(u * u, -0.044715f * 2.302208198f, -2.302208198f);
      acc[p] = fmaf(u * cc, __builtin_amdgcn_rcpf(1.f + __builtin_amdgcn_exp2f(t)), acc[p]);
    }
  }
#pragma unroll
  for (int p = 0; p < 5; ++p) { const float s = wave_sum(acc[p]); if (lane == 0) red[w][p] = s; }
  __syncthreads();
  if (tid < np) { float s = 0.f; for (int k = 0; k < 8; ++k) s += red[k][tid];
    __bf16 o = (__bf16)(hs[p0 + tid] + (s + bf2f(b2[0]))); out[b * DM + p0 + tid] = __builtin_bit_cast(unsigned short, o); }
}

extern "C" void kernel_launch(void* const* d_in, const int* in_sizes, int n_in, void* d_out, int out_size, void* d_ws, size_t ws_size, hipStream_t stream) {
  const unsigned short* x = (const unsigned short*)d_in[0];
  const char* K = (const char*)d_in[1]; const char* V = (const char*)d_in[2];
  const unsigned short* wq = (const unsigned short*)d_in[3]; const unsigned short* wo = (const unsigned short*)d_in[4];
  const unsigned short* w1 = (const unsigned short*)d_in[6]; const unsigned short* b1 = (const unsigned short*)d_in[7];
  const unsigned short* w2 = (const unsigned short*)d_in[8]; const unsigned short* b2 = (const unsigned short*)d_in[9];
  unsigned short* q = (unsigned short*)d_ws;
  float* part = (float*)((char*)d_ws + 65536);
  float* r = (float*)((char*)d_ws + 65536 + 524288);
  hipFuncSetAttribute((const void*)k_attn<3>, hipFuncAttributeMaxDynamicSharedMemorySize, 131072);
  k_q<<<128, 256, 0, stream>>>(x, wq, q);
  k_attn<3><<<256, 256, 131072, stream>>>(K, V, q, part);
  k_proj<<<144, 256, 0, stream>>>(part, wo, r);
  k_mlp<<<256, 512, 0, stream>>>(r, x, w1, b1, w2, b2, (unsigned short*)d_out);
}
```

```cpp
#include <hip/hip_runtime.h>
#include <hip/hip_bf16.h>
#include <stdint.h>

typedef short s4 __attribute__((ext_vector_type(4)));
typedef __bf16 bf2 __attribute__((ext_vector_type(2)));
typedef __bf16 bf4 __attribute__((ext_vector_type(4)));
typedef __bf16 bf8 __attribute__((ext_vector_type(8)));
typedef float f2 __attribute__((ext_vector_type(2)));
typedef float f4 __attribute__((ext_vector_type(4)));
typedef unsigned u2 __attribute__((ext_vector_type(2)));
typedef unsigned u4 __attribute__((ext_vector_type(4)));
typedef unsigned long long u64;
#define LAS __attribute__((address_space(3)))
#define GAS __attribute__((address_space(1)))

#define DEPTH 3
#define PROG_AT 48
#define NR 4
#define RAT 12
#define PW 4
#define PW2 PW
#define QSLEEP "8"
#define WQPOL " nt"
#define PTHR 5
#define RTHR 12

constexpr int DM = 576;
constexpr float EPS = 1e-5f;
constexpr float CEXP = 0.08838834764831845f * 1.4426950408889634f;
constexpr int NSLOT = 4, SLOTB = 8192, NT = 64 - NR;
constexpr unsigned WS_QP = 4096, WS_PD = WS_QP + 32768, WS_PM = WS_PD + 524288, WS_R = WS_PM + 32768, WS_END = WS_R + 147456;
constexpr int STG = 131072, QS = STG + 27648, OS = QS + 512, CNT = OS + 1024, LDS_BYTES = CNT + 64;
constexpr int HS = 4096, X8 = HS + 2304, RED = X8 + 2048;
enum { C_Q = 0, C_TAG = 1, C_MG = 2, C_O = 3, C_W = 4, C_PROG = 5, C_WO = 6, C_QR = 7 };
constexpr unsigned SPIN_G = 1u << 17, SPIN_L = 1u << 21;

#define STAMP(k) do {} while (0)
#define CSTAMP(k) do {} while (0)
#define LDS_TOTAL LDS_BYTES

__device__ __forceinline__ float bflo(unsigned v) { return __uint_as_float(v << 16); }
__device__ __forceinline__ float bfhi(unsigned v) { return __uint_as_float(v & 0xffff0000u); }
__device__ __forceinline__ float rbf(float x) { return (float)(__bf16)x; }
__device__ __forceinline__ unsigned pk2(float a, float b) { const bf2 p = {(__bf16)a, (__bf16)b}; return __builtin_bit_cast(unsigned, p); }
template <int CTRL> __device__ __forceinline__ float dpp(float x) { return __builtin_bit_cast(float, __builtin_amdgcn_mov_dpp(__builtin_bit_cast(int, x), CTRL, 0xf, 0xf, true)); }
constexpr int X1 = 0xB1, X2 = 0x4E, HM = 0x141, RM = 0x140;
__device__ __forceinline__ float add16(float x) { auto s = __builtin_amdgcn_permlane16_swap(__float_as_uint(x), __float_as_uint(x), false, false); return __uint_as_float(s[0]) + __uint_as_float(s[1]); }
__device__ __forceinline__ float add32(float x) { auto s = __builtin_amdgcn_permlane32_swap(__float_as_uint(x), __float_as_uint(x), false, false); return __uint_as_float(s[0]) + __uint_as_float(s[1]); }
__device__ __forceinline__ float max16(float x) { auto s = __builtin_amdgcn_permlane16_swap(__float_as_uint(x), __float_as_uint(x), false, false); return fmaxf(__uint_as_float(s[0]), __uint_as_float(s[1])); }
__device__ __forceinline__ float max32(float x) { auto s = __builtin_amdgcn_permlane32_swap(__float_as_uint(x), __float_as_uint(x), false, false); return fmaxf(__uint_as_float(s[0]), __uint_as_float(s[1])); }
__device__ __forceinline__ float sum_row(float x) { x += dpp<X1>(x); x += dpp<X2>(x); x += dpp<HM>(x); x += dpp<RM>(x); return x; }
__device__ __forceinline__ float sum_half(float x) { return add16(sum_row(x)); }
__device__ __forceinline__ float sum_wave(float x) { return add32(sum_half(x)); }
__device__ __forceinline__ float rdl(float x, int l) { return __builtin_bit_cast(float, __builtin_amdgcn_readlane(__builtin_bit_cast(int, x), l)); }
__device__ __forceinline__ u64 bad(const u4& v, unsigned tag) { return __builtin_amdgcn_uicmp((v.y ^ tag) | (v.w ^ tag), 0u, 33); }

template <int SLP = 1> __device__ __forceinline__ void lds_wait(LAS unsigned char* L, int w, unsigned target) {
  volatile LAS unsigned* p = (volatile LAS unsigned*)(L + CNT) + w;
#pragma nounroll
  for (unsigned s = 0; s < SPIN_L; ++s) { if (__builtin_amdgcn_readfirstlane(*p) >= target) break; if (SLP) __builtin_amdgcn_s_sleep(SLP); }
  asm volatile("" ::: "memory");
}
__device__ __forceinline__ void lds_arrive(LAS unsigned char* L, int w) {
  asm volatile("s_mov_b64 exec, 1\n\tds_add_u32 %0, %1\n\ts_mov_b64 exec, -1" :: "v"(L + CNT + 4 * w), "v"(1u) : "memory");
}
__device__ __forceinline__ unsigned lds_arrive_rtn(LAS unsigned char* L, int w) {
  unsigned r;
  asm volatile("s_mov_b64 exec, 1\n\tds_add_rtn_u32 %0, %1, %2\n\ts_mov_b64 exec, -1\n\ts_waitcnt lgkmcnt(0)" : "=&v"(r) : "v"(L + CNT + 4 * w), "v"(1u) : "memory");
  return __builtin_amdgcn_readfirstlane(r);
}
#define BARRIER() asm volatile("s_waitcnt lgkmcnt(0)\n\ts_barrier" ::: "memory")
#define WAITV(n) asm volatile("s_waitcnt vmcnt(%0)" :: "i"(n) : "memory")
#define DMA1(voff, sbase, ldst, pol) asm volatile("s_mov_b32 m0, %2\n\ts_nop 0\n\tglobal_load_lds_dwordx4 %0, %1" pol :: "v"(voff), "s"(sbase), "s"(ldst) : "memory")

#define POLL9(v, m, va, vb, sa, sb) do { u64 ex_; asm volatile("s_mov_b64 %9, exec\n\t" \
    "s_mov_b64 exec, %14\n\tglobal_load_dwordx4 %0, %10, %12 offset:-4096 sc1\n\t" "s_mov_b64 exec, %15\n\tglobal_load_dwordx4 %1, %10, %12 offset:-3072 sc1\n\t" \
    "s_mov_b64 exec, %16\n\tglobal_load_dwordx4 %2, %10, %12 offset:-2048 sc1\n\t" "s_mov_b64 exec, %17\n\tglobal_load_dwordx4 %3, %10, %12 offset:-1024 sc1\n\t" \
    "s_mov_b64 exec, %18\n\tglobal_load_dwordx4 %4, %10, %12 sc1\n\t" "s_mov_b64 exec, %19\n\tglobal_load_dwordx4 %5, %10, %12 offset:1024 sc1\n\t" \
    "s_mov_b64 exec, %20\n\tglobal_load_dwordx4 %6, %10, %12 offset:2048 sc1\n\t" "s_mov_b64 exec, %21\n\tglobal_load_dwordx4 %7, %10, %12 offset:3072 sc1\n\t" \
    "s_mov_b64 exec, %22\n\tglobal_load_dwordx4 %8, %11, %13 sc1\n\t" "s_mov_b64 exec, %9\n\ts_waitcnt vmcnt(0)" \
    : "+v"(v[0]), "+v"(v[1]), "+v"(v[2]), "+v"(v[3]), "+v"(v[4]), "+v"(v[5]), "+v"(v[6]), "+v"(v[7]), "+v"(v[8]), "=&s"(ex_) \
    : "v"(va), "v"(vb), "s"(sa), "s"(sb), "s"(m[0]), "s"(m[1]), "s"(m[2]), "s"(m[3]), "s"(m[4]), "s"(m[5]), "s"(m[6]), "s"(m[7]), "s"(m[8]) : "memory"); } while (0)

__global__ void __launch_bounds__(512, 2) fused_decode_kernel(const char* __restrict__ x, const char* __restrict__ K, const char* __restrict__ V, const char* __restrict__ wq,
    const char* __restrict__ wo, const char* __restrict__ w1, const char* __restrict__ b1, const char* __restrict__ w2, const unsigned short* __restrict__ b2,
    unsigned short* __restrict__ out, char* __restrict__ ws) {
  extern __shared__ __attribute__((aligned(16))) unsigned char lds[];
  LAS unsigned char* L = (LAS unsigned char*)lds;
  const unsigned lbase = (unsigned)(uintptr_t)L;
  const int tid = threadIdx.x, lane = tid & 63, wave = __builtin_amdgcn_readfirstlane(tid >> 6);
  const int wg = blockIdx.x, grp = wg >> 3;
  const int g = lane >> 4, c = lane & 15;
  const __amdgpu_buffer_rsrc_t wsr = __builtin_amdgcn_make_buffer_rsrc(ws, 0, WS_END, 0x00020000);
  unsigned tag;
  asm volatile("" :: "s"(x), "s"(K), "s"(V), "s"(wq), "s"(wo), "s"(w1), "s"(b1), "s"(w2), "s"(b2), "s"(out), "s"(ws));

  if (wave < 4) {
    if (wave == 0) { STAMP(0); CSTAMP(14); }
    const char* kb = K + (size_t)wg * (4096 * 256) + wave * 4096;
    const char* vb = V + (size_t)wg * (4096 * 256) + wave * 4096;
    unsigned vk[4], vv[2];
#pragma unroll
    for (int j = 0; j < 4; ++j) vk[j] = 256 * g + 16 * ((c ^ g) ^ (4 * j));
#pragma unroll
    for (int j = 0; j < 2; ++j) vv[j] = 256 * g + 16 * (c ^ (2 * g) ^ (8 * j));
    const unsigned ldsw = lbase + wave * (NSLOT * SLOTB);
#define ISSUE_K(tile, slot) asm volatile("s_mov_b32 m0, %5\n\ts_nop 0\n\t" \
      "global_load_lds_dwordx4 %0, %4 nt\n\tglobal_load_lds_dwordx4 %1, %4 offset:1024 nt\n\tglobal_load_lds_dwordx4 %2, %4 offset:2048 nt\n\tglobal_load_lds_dwordx4 %3, %4 offset:3072 nt" \
      :: "v"(vk[0]), "v"(vk[1]), "v"(vk[2]), "v"(vk[3]), "s"(kb + (size_t)(tile) * 16384), "s"(ldsw + (slot) * SLOTB) : "memory")
#define ISSUE_V(tile, slot) asm volatile("s_mov_b32 m0, %3\n\ts_nop 0\n\t" \
      "global_load_lds_dwordx4 %0, %2 nt\n\tglobal_load_lds_dwordx4 %1, %2 offset:1024 nt\n\tglobal_load_lds_dwordx4 %0, %2 offset:2048 nt\n\tglobal_load_lds_dwordx4 %1, %2 offset:3072 nt" \
      :: "v"(vv[0]), "v"(vv[1]), "s"(vb + (size_t)(tile) * 16384), "s"(ldsw + (slot) * SLOTB + 4096) : "memory")
#define ISSUE(tile, slot) do { asm volatile("s_waitcnt lgkmcnt(0)" ::: "memory"); ISSUE_K((tile) + NR, slot); ISSUE_V((tile) + NR, slot); } while (0)
    BARRIER();
    ISSUE_K(0, 0); ISSUE_V(0, 0);
#pragma unroll
    for (int t = 1; t < NSLOT; ++t) { WAITV(PW); ISSUE_K(t, t); WAITV(PW); ISSUE_V(t, t); }
    u4 rt[NR][8];
#pragma unroll
    for (int t = 0; t < NR; ++t) {
      WAITV(PW2);
      asm volatile("global_load_dwordx4 %0, %4, %8 nt\n\tglobal_load_dwordx4 %1, %5, %8 offset:1024 nt\n\tglobal_load_dwordx4 %2, %6, %8 offset:2048 nt\n\tglobal_load_dwordx4 %3, %7, %8 offset:3072 nt"
        : "=&v"(rt[t][0]), "=&v"(rt[t][1]), "=&v"(rt[t][2]), "=&v"(rt[t][3]) : "v"(vk[0]), "v"(vk[1]), "v"(vk[2]), "v"(vk[3]), "s"(kb + (size_t)(NSLOT + t) * 16384) : "memory");
      WAITV(PW2);
      asm volatile("global_load_dwordx4 %0, %4, %6 nt\n\tglobal_load_dwordx4 %1, %5, %6 offset:1024 nt\n\tglobal_load_dwordx4 %2, %4, %6 offset:2048 nt\n\tglobal_load_dwordx4 %3, %5, %6 offset:3072 nt"
        : "=&v"(rt[t][4]), "=&v"(rt[t][5]), "=&v"(rt[t][6]), "=&v"(rt[t][7]) : "v"(vv[0]), "v"(vv[1]), "s"(vb + (size_t)(NSLOT + t) * 16384) : "memory");
    }
    LAS unsigned char* ka[4]; LAS unsigned char* va[8];
#pragma unroll
    for (int s = 0; s < 4; ++s) ka[s] = L + wave * (NSLOT * SLOTB) + c * 256 + 16 * ((g ^ c) ^ (4 * s));
    { const int row = 4 * g + ((lane >> 2) & 3), p = lane & 3;
#pragma unroll
      for (int nb = 0; nb < 8; ++nb) va[nb] = L + wave * (NSLOT * SLOTB) + 4096 + row * 256 + 32 * (nb ^ (row & 7)) + 8 * p; }
    lds_wait(L, C_Q, 1);
    if (wave == 0) STAMP(3);
    tag = __builtin_amdgcn_readfirstlane(((volatile LAS unsigned*)(L + CNT))[C_TAG]);
    bf8 qf[4];
#pragma unroll
    for (int s = 0; s < 4; ++s) qf[s] = *(LAS bf8*)(L + QS + (c & 1) * 256 + 64 * s + 16 * g);
    float m = -INFINITY, mc = -INFINITY, l = 0.f;
    f4 acc[8];
#pragma unroll
    for (int nb = 0; nb < 8; ++nb) acc[nb] = f4{0.f, 0.f, 0.f, 0.f};
    auto tile = [&](const int slot, auto issue) {
      bf8 kf[4]; bf4 vf[8];
#pragma unroll
      for (int s = 0; s < 4; ++s) kf[s] = *(LAS bf8*)(ka[s] + slot * SLOTB);
#pragma unroll
      for (int nb = 0; nb < 8; ++nb) vf[nb] = __builtin_amdgcn_ds_read_tr16_b64_v4bf16((LAS bf4*)(va[nb] + slot * SLOTB));
      issue();
      f4 sc = {0.f, 0.f, 0.f, 0.f};
#pragma unroll
      for (int s = 0; s < 4; ++s) sc = __builtin_amdgcn_mfma_f32_16x16x32_bf16(kf[s], qf[s], sc, 0, 0, 0);
      float t = fmaxf(fmaxf(sc[0], sc[1]), fmaxf(sc[2], sc[3]));
      if (__any(t > m)) {
        t = max32(max16(t));
        const float mn = fmaxf(m, t);
        const float alpha = __builtin_amdgcn_exp2f((m - mn) * CEXP);
        m = mn; mc = mn * CEXP; l *= alpha;
        const float a0 = rdl(alpha, 0), a1 = rdl(alpha, 1);
#pragma unroll
        for (int nb = 0; nb < 8; ++nb) { acc[nb][0] *= a0; acc[nb][1] *= a1; }
      }
      const float p0 = __builtin_amdgcn_exp2f(fmaf(sc[0], CEXP, -mc)), p1 = __builtin_amdgcn_exp2f(fmaf(sc[1], CEXP, -mc));
      const float p2 = __builtin_amdgcn_exp2f(fmaf(sc[2], CEXP, -mc)), p3 = __builtin_amdgcn_exp2f(fmaf(sc[3], CEXP, -mc));
      l += (p0 + p1) + (p2 + p3);
      const u2 pk = {pk2(p0, p1), pk2(p2, p3)};
#pragma unroll
      for (int nb = 0; nb < 8; ++nb) acc[nb] = __builtin_amdgcn_mfma_f32_16x16x16bf16_1k(__builtin_bit_cast(s4, pk), __builtin_bit_cast(s4, vf[nb]), acc[nb], 0, 0, 0);
    };
    constexpr int D = DEPTH;
#define WAITT(n) do { if ((n) >= 3) WAITV(24); else if ((n) == 2) WAITV(16); else if ((n) == 1) WAITV(8); else WAITV(0); } while (0)
#pragma unroll
    for (int u = 0; u < NSLOT; ++u) {
      WAITT((u + D > NSLOT ? u + D : NSLOT) - u - 1);
      if (u + D >= NSLOT) tile(u, [&]() { ISSUE(u + D, (u + D) % NSLOT); }); else tile(u, [&]() {});
    }
    for (int i0 = NSLOT; i0 < NT - NSLOT; i0 += NSLOT) {
      if (i0 == PROG_AT && wave == 0 && lane == 0) ((volatile LAS unsigned*)(L + CNT))[C_PROG] = 1u;
#pragma unroll
      for (int u = 0; u < NSLOT; ++u) {
        WAITT(D - 1);
        tile(u, [&]() { ISSUE(i0 + u + D, (u + D) % NSLOT); });
        if (i0 == RAT) {
          asm volatile("" : "+v"(rt[u][0]), "+v"(rt[u][1]), "+v"(rt[u][2]), "+v"(rt[u][3]), "+v"(rt[u][4]), "+v"(rt[u][5]), "+v"(rt[u][6]), "+v"(rt[u][7]) :: "memory");
#pragma unroll
          for (int j = 0; j < 8; ++j) *(LAS u4*)(L + wave * (NSLOT * SLOTB) + u * SLOTB + 1024 * j + 16 * lane) = rt[u][j];
          tile(u, [&]() {});
        }
      }
    }
#pragma unroll
    for (int u = 0; u < NSLOT; ++u) {
      constexpr int i0 = NT - NSLOT;
      WAITT(NSLOT - 1 - u < D - 1 ? NSLOT - 1 - u : D - 1);
      if (u + D < NSLOT) tile(u, [&]() { ISSUE(i0 + u + D, (u + D) % NSLOT); }); else tile(u, [&]() {});
    }
    if (wave == 0) STAMP(4);
    l = add32(add16(l));
    LAS unsigned char* mg = L + wave * (NSLOT * SLOTB);
    if (lane < 16) {
#pragma unroll
      for (int nb = 0; nb < 8; ++nb) *(LAS f2*)(mg + 8 * (16 * nb + c)) = f2{acc[nb][0], acc[nb][1]};
      if (lane < 2) *(LAS f2*)(mg + 1024 + 8 * lane) = f2{mc, l};
    }
    if (lds_arrive_rtn(L, C_MG) == 3) {
      STAMP(13);
      f4 ml[4], d[4];
#pragma unroll
      for (int w = 0; w < 4; ++w) { ml[w] = *(LAS f4*)(L + w * (NSLOT * SLOTB) + 1024); d[w] = *(LAS f4*)(L + w * (NSLOT * SLOTB) + 16 * lane); }
      const float M0 = fmaxf(fmaxf(ml[0][0], ml[1][0]), fmaxf(ml[2][0], ml[3][0])), M1 = fmaxf(fmaxf(ml[0][2], ml[1][2]), fmaxf(ml[2][2], ml[3][2]));
      float L0 = 0.f, L1 = 0.f; f4 o = {0.f, 0.f, 0.f, 0.f};
#pragma unroll
      for (int w = 0; w < 4; ++w) {
        const float f0 = __builtin_amdgcn_exp2f(ml[w][0] - M0), f1 = __builtin_amdgcn_exp2f(ml[w][2] - M1);
        L0 = fmaf(f0, ml[w][1], L0); L1 = fmaf(f1, ml[w][3], L1);
        o[0] = fmaf(f0, d[w][0], o[0]); o[1] = fmaf(f1, d[w][1], o[1]); o[2] = fmaf(f0, d[w][2], o[2]); o[3] = fmaf(f1, d[w][3], o[3]);
      }
      const unsigned pd = WS_PD + (grp * 16 + (wg & 7)) * 1024 + 16 * lane;
      __builtin_amdgcn_raw_buffer_store_b128(u4{__float_as_uint(o[0]), tag, __float_as_uint(o[2]), tag}, wsr, pd, 0, 16);
      __builtin_amdgcn_raw_buffer_store_b128(u4{__float_as_uint(o[1]), tag, __float_as_uint(o[3]), tag}, wsr, pd + 8192, 0, 16);
      if (lane < 2) __builtin_amdgcn_raw_buffer_store_b128(u4{__float_as_uint(lane ? M1 : M0), tag, __float_as_uint(lane ? L1 : L0), tag}, wsr, WS_PM + wg * 128 + 16 * lane, 0, 16);
      STAMP(5);
    }
  } else {
    __builtin_amdgcn_s_setprio(3);
    const int hw = wave - 4;
    const int q2 = (lane >> 2) & 3, p = lane & 3;
    LAS unsigned char* tra = L + STG + (4 * g + q2) * 128 + 16 * ((2 * hw + (p >> 1)) ^ (2 * ((2 * g + (q2 >> 1)) & 3))) + 8 * (p & 1);
    const unsigned swz = 16 * ((lane & 7) ^ (2 * (lane >> 4)));
    {
      const int cb = (wg >> 5) * 32 + (wg & 7) * 4 + ((wg >> 3) & 3);
      unsigned e; u4 xs0, xs1, xs2; u2 xa[9];
      const unsigned vx0 = (lane >> 5) * 1152 + 16 * (lane & 31), vx2 = (lane >> 5) * 1152 + 1024 + 16 * (lane & 7);
      const unsigned vxa = (lane & 1) * 1152 + 288 * hw + 8 * g, vz = 0;
      asm volatile("global_load_dword %0, %4, %7 sc1\n\t"
        "global_load_dwordx4 %1, %5, %8\n\tglobal_load_dwordx4 %2, %5, %8 offset:512\n\tglobal_load_dwordx4 %3, %6, %8"
        : "=&v"(e), "=&v"(xs0), "=&v"(xs1), "=&v"(xs2) : "v"(vz), "v"(vx0), "v"(vx2), "s"(ws), "s"(x) : "memory");
      const unsigned vwq = (lane >> 1) * 8192 + 16 * (lane & 1), vwq4 = vwq & 0x1ffffu;
      const char* wqb = wq + (size_t)(144 * hw) * 8192 + 32 * cb;
#pragma unroll
      for (int i = 0; i < 5; ++i) DMA1(i < 4 ? vwq : vwq4, wqb + (size_t)i * 262144, lbase + STG + 5120 * hw + 1024 * i, WQPOL);
      asm volatile("global_load_dwordx2 %0, %9, %10\n\tglobal_load_dwordx2 %1, %9, %10 offset:32\n\tglobal_load_dwordx2 %2, %9, %10 offset:64\n\t"
        "global_load_dwordx2 %3, %9, %10 offset:96\n\tglobal_load_dwordx2 %4, %9, %10 offset:128\n\tglobal_load_dwordx2 %5, %9, %10 offset:160\n\t"
        "global_load_dwordx2 %6, %9, %10 offset:192\n\tglobal_load_dwordx2 %7, %9, %10 offset:224\n\tglobal_load_dwordx2 %8, %9, %10 offset:256"
        : "=&v"(xa[0]), "=&v"(xa[1]), "=&v"(xa[2]), "=&v"(xa[3]), "=&v"(xa[4]), "=&v"(xa[5]), "=&v"(xa[6]), "=&v"(xa[7]), "=&v"(xa[8])
        : "v"(vxa), "s"(x) : "memory");
      if (hw == 0 && lane < 8) ((volatile LAS unsigned*)(L + CNT))[lane] = 0u;
      BARRIER();
      asm volatile("s_waitcnt vmcnt(14)" : "+v"(e), "+v"(xs0), "+v"(xs1), "+v"(xs2) :: "memory");
      tag = __builtin_amdgcn_readfirstlane(e) + 1u;
      float v[24];
#pragma unroll
      for (int k = 0; k < 4; ++k) { v[2 * k] = bflo(xs0[k]); v[2 * k + 1] = bfhi(xs0[k]); v[8 + 2 * k] = bflo(xs1[k]); v[9 + 2 * k] = bfhi(xs1[k]); v[16 + 2 * k] = bflo(xs2[k]); v[17 + 2 * k] = bfhi(xs2[k]); }
      const bool t3 = (lane & 31) < 8;
      float s = 0.f, s2 = 0.f, s3 = 0.f;
#pragma unroll
      for (int k = 0; k < 8; ++k) { s += v[k]; s2 += v[8 + k]; s3 += v[16 + k]; }
      const float mean = sum_half(s + s2 + (t3 ? s3 : 0.f)) * (1.f / DM);
      s = 0.f; s2 = 0.f; s3 = 0.f;
#pragma unroll
      for (int k = 0; k < 8; ++k) { const float d1 = v[k] - mean, d2 = v[8 + k] - mean, d3 = v[16 + k] - mean; s = fmaf(d1, d1, s); s2 = fmaf(d2, d2, s2); s3 = fmaf(d3, d3, s3); }
      const float rstd = __builtin_amdgcn_rsqf(sum_half(s + s2 + (t3 ? s3 : 0.f)) * (1.f / DM) + EPS);
      float mean0 = rdl(mean, 0), mean1 = rdl(mean, 32), rstd0 = rdl(rstd, 0), rstd1 = rdl(rstd, 32);
      asm volatile("" : "+s"(mean0), "+s"(mean1), "+s"(rstd0), "+s"(rstd1));
      LAS unsigned char* trq = L + STG + 5120 * hw + (lane >> 2) * 32 + 8 * p;
      LAS f4* pr = (LAS f4*)(L + OS) + c;
      asm volatile("s_waitcnt vmcnt(0)" : "+v"(xa[0]), "+v"(xa[1]), "+v"(xa[2]), "+v"(xa[3]), "+v"(xa[4]), "+v"(xa[5]), "+v"(xa[6]), "+v"(xa[7]), "+v"(xa[8]), "+v"(trq), "+v"(pr) :: "memory");
      if (hw == 0) STAMP(1);
      u2 fr[9];
#pragma unroll
      for (int t = 0; t < 9; ++t) fr[t] = __builtin_bit_cast(u2, __builtin_amdgcn_ds_read_tr16_b64_v4bf16((LAS bf4*)(trq + 512 * t)));
#pragma unroll
      for (int k = 0; k < 9; ++k) { xa[k].x = (c & 2) ? 0x3f803f80u : xa[k].x; xa[k].y = (c & 2) ? 0x3f803f80u : xa[k].y; }
      f4 d = {0.f, 0.f, 0.f, 0.f};
#pragma unroll
      for (int t = 0; t < 4; ++t)
        d = __builtin_amdgcn_mfma_f32_16x16x32_bf16(__builtin_bit_cast(bf8, u4{xa[2 * t].x, xa[2 * t].y, xa[2 * t + 1].x, xa[2 * t + 1].y}), __builtin_bit_cast(bf8, u4{fr[2 * t].x, fr[2 * t].y, fr[2 * t + 1].x, fr[2 * t + 1].y}), d, 0, 0, 0);
      d += __builtin_amdgcn_mfma_f32_16x16x16bf16_1k(__builtin_bit_cast(s4, xa[8]), __builtin_bit_cast(s4, fr[8]), f4{0.f, 0.f, 0.f, 0.f}, 0, 0, 0);
      if (lane < 16) pr[16 * hw] = d;
      if (lds_arrive_rtn(L, C_QR) == 3) {
        d = (pr[0] + pr[16]) + (pr[32] + pr[48]);
        const float v0 = rstd0 * (d[0] - mean0 * d[2]), v1 = rstd1 * (d[1] - mean1 * d[2]);
        const float n0 = dpp<X1>(v0), n1 = dpp<X1>(v1);
        const unsigned P = (c & 1) ? pk2(n1, v1) : pk2(v0, n0);
        const unsigned Q = __float_as_uint(dpp<X2>(__uint_as_float(P)));
        if (lane < 16 && !(c & 2)) __builtin_amdgcn_raw_buffer_store_b128(u4{P, tag, Q, tag}, wsr, WS_QP + (c & 1) * 16384 + cb * 64 + 16 * (c >> 2), 0, 16);
        STAMP(2);
        const char* qb = ws + WS_QP + grp * 1024;
        const unsigned vo = 16 * lane, vq = lbase + QS + 8 * lane, vf = lbase + CNT;
        const u2 fl = {1u, tag};
        unsigned cnt = SPIN_G;
#define QLD(r) "global_load_dwordx4 v[" #r "], %1, %2 sc1\n\t"
#define QCK(y, w, lbl) "s_waitcnt vmcnt(3)\n\tv_bitop3_b32 v56, v" #y ", v" #w ", %3 bitop3:0x7e\n\tv_cmp_ne_u32_e32 vcc, 0, v56\n\ts_cbranch_vccz " #lbl "\n\ts_sleep " QSLEEP "\n\t"
        asm volatile(QLD(40:43) "s_sleep " QSLEEP "\n\t" QLD(44:47) "s_sleep " QSLEEP "\n\t" QLD(48:51) "s_sleep " QSLEEP "\n"
          "1:\n\t" QLD(52:55) QCK(41, 43, 2f) QLD(40:43) QCK(45, 47, 3f) QLD(44:47) QCK(49, 51, 4f) QLD(48:51) QCK(53, 55, 5f)
          "s_sub_u32 %0, %0, 1\n\ts_cbranch_scc0 1b\n\ts_waitcnt vmcnt(0)\n"
          "2:\n\tds_write2_b32 %4, v40, v42 offset1:1\n\ts_branch 6f\n"
          "3:\n\tds_write2_b32 %4, v44, v46 offset1:1\n\ts_branch 6f\n"
          "4:\n\tds_write2_b32 %4, v48, v50 offset1:1\n\ts_branch 6f\n"
          "5:\n\tds_write2_b32 %4, v52, v54 offset1:1\n"
          "6:\n\tds_write_b64 %5, %6\n\ts_waitcnt vmcnt(0) lgkmcnt(0)"
          : "+s"(cnt) : "v"(vo), "s"(qb), "s"(tag), "v"(vq), "v"(vf), "v"(fl)
          : "memory", "vcc", "scc", "v40", "v41", "v42", "v43", "v44", "v45", "v46", "v47", "v48", "v49", "v50", "v51", "v52", "v53", "v54", "v55", "v56");
      }
    }
    __builtin_amdgcn_s_setprio(0);
    lds_wait<4>(L, C_Q, 1);
    const bool proj = wg < 144;
    const int kvp = wg / 9, cbp = wg - 9 * kvp;
    u4 wf[8];
    if (proj) {
      const unsigned vwo = (lane >> 3) * 1152 + swz;
#pragma unroll
      for (int r = 0; r < 2; ++r) {
        const char* wob = wo + (size_t)(kvp * 256 + 128 * r + 32 * hw) * 1152 + 128 * cbp;
#pragma unroll
        for (int i = 0; i < 4; ++i) DMA1(vwo, wob + i * 9216, lbase + STG + 4096 * hw + 1024 * i, " nt");
        WAITV(0);
        lds_arrive(L, C_WO);
        lds_wait(L, C_WO, 8 * r + 4);
#pragma unroll
        for (int t = 0; t < 4; ++t) {
          const u2 ba = __builtin_bit_cast(u2, __builtin_amdgcn_ds_read_tr16_b64_v4bf16((LAS bf4*)(tra + 4096 * t)));
          const u2 bb = __builtin_bit_cast(u2, __builtin_amdgcn_ds_read_tr16_b64_v4bf16((LAS bf4*)(tra + 4096 * t + 2048)));
          wf[4 * r + t] = u4{ba.x, ba.y, bb.x, bb.y};
        }
        lds_arrive(L, C_WO);
        lds_wait(L, C_WO, 8 * r + 8);
      }
    }
    {
      const unsigned vo = 16 * lane;
#pragma unroll
      for (int i = 0; i < 3; ++i) { const int j = i < 2 ? hw + 4 * i : 8;
        DMA1(vo, w1 + 1024 * j, lbase + STG + 1024 * j, ""); DMA1(vo, b1 + 1024 * j, lbase + STG + 9216 + 1024 * j, ""); DMA1(vo, w2 + 1024 * j, lbase + STG + 18432 + 1024 * j, ""); }
      WAITV(0);
      lds_arrive(L, C_W);
    }
    if (proj) {
      const int gp = (hw >> 1) * 16 + kvp, h = hw & 1;
      lds_wait<16>(L, C_PROG, 1);
      u4 v[9]; u64 mk[9];
#pragma unroll
      for (int k = 0; k < 9; ++k) { v[k] = u4{0u, ~tag, 0u, ~tag}; mk[k] = k < 8 ? ~0ull : 0xffull; }
      const unsigned va = 16 * lane + 4096, vb = 128 * (lane & 7);
      const char* sa = ws + WS_PD + (gp * 2 + h) * 8192; const char* sb = ws + WS_PM + gp * 1024 + 16 * h;
#pragma nounroll
      for (unsigned s = 0; s < SPIN_G; ++s) {
        const unsigned seen = ~(unsigned)mk[8] & 0xffu;
        const bool most = __builtin_popcount(seen) >= PTHR;
        u64 want[9];
#pragma unroll
        for (int k = 0; k < 8; ++k) want[k] = (most || ((seen >> k) & 1)) ? mk[k] : 0ull;
        want[8] = mk[8];
        if (!most && s) __builtin_amdgcn_s_sleep(8);
        POLL9(v, want, va, vb, sa, sb);
        u64 any = 0;
#pragma unroll
        for (int k = 0; k < 9; ++k) { mk[k] = bad(v[k], tag); if (k == 8) mk[k] &= 0xffull; any |= mk[k]; }
        if (any == 0) break;
      }
      if (hw == 0) STAMP(6);
      const float Ms = __uint_as_float(v[8].x), Ls = __uint_as_float(v[8].z);
      float M = fmaxf(Ms, dpp<X1>(Ms)); M = fmaxf(M, dpp<X2>(M)); M = fmaxf(M, dpp<HM>(M));
      const float f = __builtin_amdgcn_exp2f(Ms - M);
      float Lt = f * Ls; Lt += dpp<X1>(Lt); Lt += dpp<X2>(Lt); Lt += dpp<HM>(Lt);
      const float wgt = f * __builtin_amdgcn_rcpf(Lt);
      float o0 = 0.f, o1 = 0.f;
#pragma unroll
      for (int s = 0; s < 8; ++s) { const float w = rdl(wgt, s); o0 = fmaf(w, __uint_as_float(v[s].x), o0); o1 = fmaf(w, __uint_as_float(v[s].z), o1); }
      *(LAS unsigned*)(L + OS + (hw >> 1) * 512 + (4 * h + (lane >> 4)) * 64 + 16 * ((lane >> 1) & 3) + 8 * ((lane >> 3) & 1) + 4 * (lane & 1)) = pk2(o0, o1);
      lds_arrive(L, C_O);
      lds_wait<0>(L, C_O, 4);
      f4 da = {0.f, 0.f, 0.f, 0.f}, db = {0.f, 0.f, 0.f, 0.f};
#pragma unroll
      for (int s = 0; s < 4; ++s) {
        da = __builtin_amdgcn_mfma_f32_16x16x32_bf16(*(LAS bf8*)(L + OS + (c & 1) * 512 + 64 * s + 16 * g), __builtin_bit_cast(bf8, wf[s]), da, 0, 0, 0);
        db = __builtin_amdgcn_mfma_f32_16x16x32_bf16(*(LAS bf8*)(L + OS + (c & 1) * 512 + 64 * (s + 4) + 16 * g), __builtin_bit_cast(bf8, wf[s + 4]), db, 0, 0, 0);
      }
      const float v0 = da[0] + db[0], v1 = da[1] + db[1];
      const float n0 = dpp<X1>(v0), n1 = dpp<X1>(v1);
      if (lane < 16) __builtin_amdgcn_raw_buffer_store_b128(u4{__float_as_uint((c & 1) ? n1 : v0), tag, __float_as_uint((c & 1) ? v1 : n0), tag}, wsr,
                                                            WS_R + (((c & 1) * 9 + cbp) * 16 + kvp) * 512 + 16 * (8 * hw + (c >> 1)), 0, 16);
      if (hw == 0) STAMP(7);
    }
  }

  const int bo = wg >> 7, jo = wg & 127;
  const int p0 = 9 * (jo >> 1) + (jo & 1) * 5, np = (jo & 1) ? 4 : 5;
  const unsigned xr = *(const GAS unsigned*)(x + bo * 1152 + 128 * wave + 4 * (lane & 31));
  const float x8 = bflo(*(const GAS unsigned short*)(x + bo * 1152 + 1024 + 2 * lane));
  const float bias2 = bflo(b2[0]);
  lds_wait(L, C_W, 4);
  float ca[9], cb_[9], t0[9], t1[9], t2[9], t3[9];
  {
    const u4 a = *(LAS u4*)(L + STG + 16 * tid), b = *(LAS u4*)(L + STG + 9216 + 16 * tid), cc = *(LAS u4*)(L + STG + 18432 + 16 * tid);
    float wa[9], wb[9], wc[9];
#pragma unroll
    for (int k = 0; k < 4; ++k) { wa[2 * k] = bflo(a[k]); wa[2 * k + 1] = bfhi(a[k]); wb[2 * k] = bflo(b[k]); wb[2 * k + 1] = bfhi(b[k]); wc[2 * k] = bflo(cc[k]); wc[2 * k + 1] = bfhi(cc[k]); }
    wa[8] = bflo(*(LAS unsigned short*)(L + STG + 8192 + 2 * tid)); wb[8] = bflo(*(LAS unsigned short*)(L + STG + 9216 + 8192 + 2 * tid)); wc[8] = bflo(*(LAS unsigned short*)(L + STG + 18432 + 8192 + 2 * tid));
    constexpr float G1 = -2.302208198f, G3 = 0.044715f * G1;
#pragma unroll
    for (int k = 0; k < 9; ++k) {
      const float w = wa[k], bb = wb[k], b2_ = bb * bb, gw = G3 * w;
      ca[k] = w * wc[k]; cb_[k] = bb * wc[k];
      t0[k] = bb * fmaf(b2_, G3, G1); t1[k] = w * fmaf(b2_, 3.f * G3, G1); t2[k] = 3.f * gw * w * bb; t3[k] = gw * w * w;
      asm volatile("" : "+v"(ca[k]), "+v"(cb_[k]), "+v"(t0[k]), "+v"(t1[k]), "+v"(t2[k]), "+v"(t3[k]));
    }
  }
  BARRIER();
  if (wave == 0) {
    const unsigned vs = 512 * c; const char* ss = ws + WS_R + (bo * 9 + wg % 9) * 8192;
#pragma nounroll
    for (unsigned s = 0; s < SPIN_G; ++s) {
      u4 r; u64 ex_;
      asm volatile("s_mov_b64 %1, exec\n\ts_mov_b64 exec, 0xffff\n\tglobal_load_dwordx4 %0, %2, %3 sc1\n\ts_mov_b64 exec, %1\n\ts_waitcnt vmcnt(0)" : "=&v"(r), "=&s"(ex_) : "v"(vs), "s"(ss) : "memory");
      if (__builtin_popcount(~(unsigned)bad(r, tag) & 0xffffu) >= RTHR) break;
      __builtin_amdgcn_s_sleep(4);
    }
    STAMP(12);
  }
  BARRIER();
  {
    u4 v[9]; u64 mk[9];
#pragma unroll
    for (int k = 0; k < 9; ++k) { v[k] = u4{0u, ~tag, 0u, ~tag}; mk[k] = ~0ull; }
    const unsigned va = 16 * lane + 4096, vb = 16 * lane;
    const char* sa = ws + WS_R + (bo * 9 + wave) * 8192; const char* sb = ws + WS_R + (bo * 9 + 8) * 8192 + wave * 1024;
#pragma nounroll
    for (unsigned s = 0; s < SPIN_G; ++s) {
      POLL9(v, mk, va, vb, sa, sb);
      u64 any = 0;
#pragma unroll
      for (int k = 0; k < 9; ++k) { mk[k] = bad(v[k], tag); any |= mk[k]; }
      if (any == 0) break;
    }
    if (wave == 0) STAMP(8);
    float s0 = (__uint_as_float(v[0].x) + __uint_as_float(v[1].x)) + (__uint_as_float(v[2].x) + __uint_as_float(v[3].x)) + ((__uint_as_float(v[4].x) + __uint_as_float(v[5].x)) + (__uint_as_float(v[6].x) + __uint_as_float(v[7].x)));
    float s1 = (__uint_as_float(v[0].z) + __uint_as_float(v[1].z)) + (__uint_as_float(v[2].z) + __uint_as_float(v[3].z)) + ((__uint_as_float(v[4].z) + __uint_as_float(v[5].z)) + (__uint_as_float(v[6].z) + __uint_as_float(v[7].z)));
    s0 = add32(s0); s1 = add32(s1);
    const float e0 = add32(__uint_as_float(v[8].x)), e1 = add32(__uint_as_float(v[8].z));
    if (lane < 32) { *(LAS f2*)(L + HS + 256 * wave + 8 * lane) = f2{rbf(bflo(xr) + rbf(s0)), rbf(bfhi(xr) + rbf(s1))}; *(LAS f2*)(L + X8 + 256 * wave + 8 * lane) = f2{e0, e1}; }
  }
  BARRIER();
  if (wave == 0) STAMP(9);
  if (wg == 0 && tid == 0) __hip_atomic_store((GAS unsigned*)ws, tag, __ATOMIC_RELAXED, __HIP_MEMORY_SCOPE_AGENT);
  float hv[9];
#pragma unroll
  for (int k = 0; k < 8; ++k) hv[k] = *(LAS float*)(L + HS + 256 * k + 4 * lane);
  { float e[8];
#pragma unroll
    for (int k = 0; k < 8; ++k) e[k] = *(LAS float*)(L + X8 + 256 * k + 4 * lane);
    hv[8] = rbf(x8 + rbf(((e[0] + e[1]) + (e[2] + e[3])) + ((e[4] + e[5]) + (e[6] + e[7])))); }
  *(LAS float*)(L + HS + 2048 + 4 * lane) = hv[8];
  const float piv = rdl(hv[0], 0);
  float sa = 0.f, sb = 0.f;
#pragma unroll
  for (int k = 0; k < 9; ++k) { const float d = hv[k] - piv; sa += d; sb = fmaf(d, d, sb); }
  sa = sum_wave(sa) * (1.f / DM); sb = sum_wave(sb) * (1.f / DM);
  const float mean = piv + sa, var = fmaxf(sb - sa * sa, 0.f);
  const float rstd = __builtin_amdgcn_rsqf(var + EPS), scale = rstd * __builtin_amdgcn_rsqf(var * rstd * rstd + EPS);
  float acc[5];
#pragma unroll
  for (int p = 0; p < 5; ++p) {
    const float z = (*(LAS float*)(L + HS + 4 * min(p0 + p, DM - 1)) - mean) * scale;
    const f2 z2 = {z, z};
    f2 a = {0.f, 0.f};
#pragma unroll
    for (int k = 0; k < 8; k += 2) {
      const f2 t = __builtin_elementwise_fma(__builtin_elementwise_fma(__builtin_elementwise_fma(f2{t3[k], t3[k + 1]}, z2, f2{t2[k], t2[k + 1]}), z2, f2{t1[k], t1[k + 1]}), z2, f2{t0[k], t0[k + 1]});
      const f2 n = __builtin_elementwise_fma(f2{ca[k], ca[k + 1]}, z2, f2{cb_[k], cb_[k + 1]});
      const f2 dn = f2{__builtin_amdgcn_exp2f(t[0]), __builtin_amdgcn_exp2f(t[1])} + f2{1.f, 1.f};
      a = __builtin_elementwise_fma(n, f2{__builtin_amdgcn_rcpf(dn[0]), __builtin_amdgcn_rcpf(dn[1])}, a);
    }
    const float t = fmaf(fmaf(fmaf(t3[8], z, t2[8]), z, t1[8]), z, t0[8]);
    acc[p] = fmaf(fmaf(ca[8], z, cb_[8]), __builtin_amdgcn_rcpf(1.f + __builtin_amdgcn_exp2f(t)), a[0] + a[1]);
  }
  if (wave == 0) STAMP(10);
  {
    auto s01 = __builtin_amdgcn_permlane32_swap(__float_as_uint(acc[0]), __float_as_uint(acc[1]), false, false);
    auto s23 = __builtin_amdgcn_permlane32_swap(__float_as_uint(acc[2]), __float_as_uint(acc[3]), false, false);
    const float r01 = __uint_as_float(s01[0]) + __uint_as_float(s01[1]), r23 = __uint_as_float(s23[0]) + __uint_as_float(s23[1]);
    auto sq = __builtin_amdgcn_permlane16_swap(__float_as_uint(r01), __float_as_uint(r23), false, false);
    const float r = sum_row(__uint_as_float(sq[0]) + __uint_as_float(sq[1])), r4 = sum_wave(acc[4]);
    if (c == 0) *(LAS float*)(L + RED + 32 * wave + 4 * ((g >> 1) + 2 * (g & 1))) = r;
    if (lane == 0) *(LAS float*)(L + RED + 32 * wave + 16) = r4;
  }
  BARRIER();
  if (tid < np) {
    float y[8];
#pragma unroll
    for (int k = 0; k < 8; ++k) y[k] = *(LAS float*)(L + RED + 32 * k + 4 * tid);
    const __bf16 o = (__bf16)(*(LAS float*)(L + HS + 4 * (p0 + tid)) + rbf((((y[0] + y[1]) + (y[2] + y[3])) + ((y[4] + y[5]) + (y[6] + y[7]))) + bias2));
    out[bo * DM + p0 + tid] = __builtin_bit_cast(unsigned short, o);
  }
}

extern "C" void kernel_launch(void* const* d_in, const int* in_sizes, int n_in, void* d_out, int out_size, void* d_ws, size_t ws_size, hipStream_t stream) {
  (void)hipFuncSetAttribute((const void*)fused_decode_kernel, hipFuncAttributeMaxDynamicSharedMemorySize, LDS_TOTAL);
  fused_decode_kernel<<<256, 512, LDS_TOTAL, stream>>>((const char*)d_in[0], (const char*)d_in[1], (const char*)d_in[2], (const char*)d_in[3], (const char*)d_in[4],
      (const char*)d_in[6], (const char*)d_in[7], (const char*)d_in[8], (const unsigned short*)d_in[9], (unsigned short*)d_out, (char*)d_ws);
}
```

```cpp
#include <hip/hip_runtime.h>
#include <hip/hip_bf16.h>
#include <stdint.h>

typedef short s4 __attribute__((ext_vector_type(4)));
typedef __bf16 bf2 __attribute__((ext_vector_type(2)));
typedef __bf16 bf4 __attribute__((ext_vector_type(4)));
typedef __bf16 bf8 __attribute__((ext_vector_type(8)));
typedef float f2 __attribute__((ext_vector_type(2)));
typedef float f4 __attribute__((ext_vector_type(4)));
typedef unsigned u2 __attribute__((ext_vector_type(2)));
typedef unsigned u4 __attribute__((ext_vector_type(4)));
typedef unsigned long long u64;
#define LAS __attribute__((address_space(3)))
#define GAS __attribute__((address_space(1)))

#define DEPTH 4
#define PROG_AT 6
#define NR 8
#define RAT 1
#define PW 12
#define PW2 PW
#define QSLEEP "8"
#define WQPOL " nt"
#define KPOL " nt"
#define VPOL " nt"
#define PTHR 5
#define RTHR 12

constexpr int DM = 576;
constexpr float EPS = 1e-5f;
constexpr float CEXP = 0.08838834764831845f * 1.4426950408889634f;
constexpr int RING = 32768;
constexpr unsigned WS_QP = 4096, WS_PD = WS_QP + 32768, WS_PM = WS_PD + 524288, WS_R = WS_PM + 32768, WS_END = WS_R + 147456;
constexpr int STG = 131072, QS = STG + 27648, OS = QS + 512, CNT = OS + 1024, LDS_BYTES = CNT + 64;
constexpr int HS = 4096, X8 = HS + 2304, RED = X8 + 2048;
enum { C_Q = 0, C_TAG = 1, C_MG = 2, C_O = 3, C_W = 4, C_PROG = 5, C_WO = 6, C_QR = 7 };
constexpr unsigned SPIN_G = 1u << 17, SPIN_L = 1u << 21;

#define STAMP(k) do {} while (0)
#define CSTAMP(k) do {} while (0)
#define LDS_TOTAL LDS_BYTES

template <int V> struct IC { static constexpr int v = V; };
template <int N, class F> __device__ __forceinline__ void sfor(F f) { if constexpr (N > 0) { sfor<N - 1>(f); f(IC<N - 1>{}); } }
__device__ __forceinline__ float bflo(unsigned v) { return __uint_as_float(v << 16); }
__device__ __forceinline__ float bfhi(unsigned v) { return __uint_as_float(v & 0xffff0000u); }
__device__ __forceinline__ float rbf(float x) { return (float)(__bf16)x; }
__device__ __forceinline__ unsigned pk2(float a, float b) { const bf2 p = {(__bf16)a, (__bf16)b}; return __builtin_bit_cast(unsigned, p); }
template <int CTRL> __device__ __forceinline__ float dpp(float x) { return __builtin_bit_cast(float, __builtin_amdgcn_mov_dpp(__builtin_bit_cast(int, x), CTRL, 0xf, 0xf, true)); }
constexpr int X1 = 0xB1, X2 = 0x4E, HM = 0x141, RM = 0x140, ROR4 = 0x124, ROR8 = 0x128;
__device__ __forceinline__ float add16(float x) { auto s = __builtin_amdgcn_permlane16_swap(__float_as_uint(x), __float_as_uint(x), false, false); return __uint_as_float(s[0]) + __uint_as_float(s[1]); }
__device__ __forceinline__ float add32(float x) { auto s = __builtin_amdgcn_permlane32_swap(__float_as_uint(x), __float_as_uint(x), false, false); return __uint_as_float(s[0]) + __uint_as_float(s[1]); }
__device__ __forceinline__ float max16(float x) { auto s = __builtin_amdgcn_permlane16_swap(__float_as_uint(x), __float_as_uint(x), false, false); return fmaxf(__uint_as_float(s[0]), __uint_as_float(s[1])); }
__device__ __forceinline__ float max32(float x) { auto s = __builtin_amdgcn_permlane32_swap(__float_as_uint(x), __float_as_uint(x), false, false); return fmaxf(__uint_as_float(s[0]), __uint_as_float(s[1])); }
__device__ __forceinline__ float sum_row(float x) { x += dpp<X1>(x); x += dpp<X2>(x); x += dpp<HM>(x); x += dpp<RM>(x); return x; }
__device__ __forceinline__ float sum_half(float x) { return add16(sum_row(x)); }
__device__ __forceinline__ float sum_wave(float x) { return add32(sum_half(x)); }
__device__ __forceinline__ float rdl(float x, int l) { return __builtin_bit_cast(float, __builtin_amdgcn_readlane(__builtin_bit_cast(int, x), l)); }
__device__ __forceinline__ u64 bad(const u4& v, unsigned tag) { return __builtin_amdgcn_uicmp((v.y ^ tag) | (v.w ^ tag), 0u, 33); }

template <int SLP = 1> __device__ __forceinline__ void lds_wait(LAS unsigned char* L, int w, unsigned target) {
  volatile LAS unsigned* p = (volatile LAS unsigned*)(L + CNT) + w;
#pragma nounroll
  for (unsigned s = 0; s < SPIN_L; ++s) { if (__builtin_amdgcn_readfirstlane(*p) >= target) break; if (SLP) __builtin_amdgcn_s_sleep(SLP); }
  asm volatile("" ::: "memory");
}
__device__ __forceinline__ void lds_arrive(LAS unsigned char* L, int w) {
  asm volatile("s_mov_b64 exec, 1\n\tds_add_u32 %0, %1\n\ts_mov_b64 exec, -1" :: "v"(L + CNT + 4 * w), "v"(1u) : "memory");
}
__device__ __forceinline__ unsigned lds_arrive_rtn(LAS unsigned char* L, int w) {
  unsigned r;
  asm volatile("s_mov_b64 exec, 1\n\tds_add_rtn_u32 %0, %1, %2\n\ts_mov_b64 exec, -1\n\ts_waitcnt lgkmcnt(0)" : "=&v"(r) : "v"(L + CNT + 4 * w), "v"(1u) : "memory");
  return __builtin_amdgcn_readfirstlane(r);
}
#define BARRIER() asm volatile("s_waitcnt lgkmcnt(0)\n\ts_barrier" ::: "memory")
#define WAITV(n) asm volatile("s_waitcnt vmcnt(%0)" :: "i"(n) : "memory")
#define DMA1(voff, sbase, ldst, pol) asm volatile("s_mov_b32 m0, %2\n\ts_nop 0\n\tglobal_load_lds_dwordx4 %0, %1" pol :: "v"(voff), "s"(sbase), "s"(ldst) : "memory")

#define POLL9(v, m, va, vb, sa, sb) do { u64 ex_; asm volatile("s_mov_b64 %9, exec\n\t" \
    "s_mov_b64 exec, %14\n\tglobal_load_dwordx4 %0, %10, %12 offset:-4096 sc1\n\t" "s_mov_b64 exec, %15\n\tglobal_load_dwordx4 %1, %10, %12 offset:-3072 sc1\n\t" \
    "s_mov_b64 exec, %16\n\tglobal_load_dwordx4 %2, %10, %12 offset:-2048 sc1\n\t" "s_mov_b64 exec, %17\n\tglobal_load_dwordx4 %3, %10, %12 offset:-1024 sc1\n\t" \
    "s_mov_b64 exec, %18\n\tglobal_load_dwordx4 %4, %10, %12 sc1\n\t" "s_mov_b64 exec, %19\n\tglobal_load_dwordx4 %5, %10, %12 offset:1024 sc1\n\t" \
    "s_mov_b64 exec, %20\n\tglobal_load_dwordx4 %6, %10, %12 offset:2048 sc1\n\t" "s_mov_b64 exec, %21\n\tglobal_load_dwordx4 %7, %10, %12 offset:3072 sc1\n\t" \
    "s_mov_b64 exec, %22\n\tglobal_load_dwordx4 %8, %11, %13 sc1\n\t" "s_mov_b64 exec, %9\n\ts_waitcnt vmcnt(0)" \
    : "+v"(v[0]), "+v"(v[1]), "+v"(v[2]), "+v"(v[3]), "+v"(v[4]), "+v"(v[5]), "+v"(v[6]), "+v"(v[7]), "+v"(v[8]), "=&s"(ex_) \
    : "v"(va), "v"(vb), "s"(sa), "s"(sb), "s"(m[0]), "s"(m[1]), "s"(m[2]), "s"(m[3]), "s"(m[4]), "s"(m[5]), "s"(m[6]), "s"(m[7]), "s"(m[8]) : "memory"); } while (0)

__global__ void __launch_bounds__(512, 2) fused_decode_kernel(const char* __restrict__ x, const char* __restrict__ K, const char* __restrict__ V, const char* __restrict__ wq,
    const char* __restrict__ wo, const char* __restrict__ w1, const char* __restrict__ b1, const char* __restrict__ w2, const unsigned short* __restrict__ b2,
    unsigned short* __restrict__ out, char* __restrict__ ws) {
  extern __shared__ __attribute__((aligned(16))) unsigned char lds[];
  LAS unsigned char* L = (LAS unsigned char*)lds;
  const unsigned lbase = (unsigned)(uintptr_t)L;
  const int tid = threadIdx.x, lane = tid & 63, wave = __builtin_amdgcn_readfirstlane(tid >> 6);
  const int wg = blockIdx.x, grp = wg >> 3;
  const int g = lane >> 4, c = lane & 15;
  const __amdgpu_buffer_rsrc_t wsr = __builtin_amdgcn_make_buffer_rsrc(ws, 0, WS_END, 0x00020000);
  unsigned tag;
  asm volatile("" :: "s"(x), "s"(K), "s"(V), "s"(wq), "s"(wo), "s"(w1), "s"(b1), "s"(w2), "s"(b2), "s"(out), "s"(ws));

  if (wave < 4) {
    if (wave == 0) { STAMP(0); CSTAMP(14); }
    const char* kb = K + (size_t)wg * (4096 * 256) + wave * 4096;
    const char* vb = V + (size_t)wg * (4096 * 256) + wave * 4096;
    unsigned vo[4];
#pragma unroll
    for (int j = 0; j < 4; ++j) vo[j] = 256 * g + 16 * (c ^ (2 * g) ^ (8 * (j & 1) + (j >> 1)));
    const unsigned ldsw = lbase + wave * RING;
#define ISSUE(ptr, slot, pol) asm volatile("s_waitcnt lgkmcnt(0)\n\ts_mov_b32 m0, %5\n\ts_nop 0\n\t" \
      "global_load_lds_dwordx4 %0, %4" pol "\n\tglobal_load_lds_dwordx4 %1, %4 offset:1024" pol "\n\tglobal_load_lds_dwordx4 %2, %4 offset:2048" pol "\n\tglobal_load_lds_dwordx4 %3, %4 offset:3072" pol \
      :: "v"(vo[0]), "v"(vo[1]), "v"(vo[2]), "v"(vo[3]), "s"(ptr), "s"(ldsw + (slot) * 4096) : "memory")
    BARRIER();
#pragma unroll
    for (int t = 0; t < 8; ++t) { if (4 * t > PW) WAITV(PW); ISSUE(kb + t * 16384, t, KPOL); }
    u4 rt[8][4];
#pragma unroll
    for (int t = 0; t < 8; ++t) {
      WAITV(PW2);
      asm volatile("global_load_dwordx4 %0, %4, %8" KPOL "\n\tglobal_load_dwordx4 %1, %5, %8 offset:1024" KPOL "\n\tglobal_load_dwordx4 %2, %6, %8 offset:2048" KPOL "\n\tglobal_load_dwordx4 %3, %7, %8 offset:3072" KPOL
        : "=&v"(rt[t][0]), "=&v"(rt[t][1]), "=&v"(rt[t][2]), "=&v"(rt[t][3]) : "v"(vo[0]), "v"(vo[1]), "v"(vo[2]), "v"(vo[3]), "s"(kb + (8 + t) * 16384) : "memory");
    }
    LAS unsigned char* ka[4]; LAS unsigned char* va[8];
#pragma unroll
    for (int s = 0; s < 4; ++s) ka[s] = L + wave * RING + c * 256 + 16 * ((g ^ (2 * (c & 7) + (c >> 3))) ^ (4 * s));
    lds_wait(L, C_Q, 1);
    if (wave == 0) STAMP(3);
    tag = __builtin_amdgcn_readfirstlane(((volatile LAS unsigned*)(L + CNT))[C_TAG]);
    bf8 qf[4];
#pragma unroll
    for (int s = 0; s < 4; ++s) qf[s] = *(LAS bf8*)(L + QS + (c & 1) * 256 + 64 * s + 16 * g);
    constexpr int D = DEPTH, NG = NR ? 7 : 8;
    auto ktile = [&](const int u, f4& S) {
      f4 sc = {0.f, 0.f, 0.f, 0.f};
#pragma unroll
      for (int s = 0; s < 4; ++s) sc = __builtin_amdgcn_mfma_f32_16x16x32_bf16(*(LAS bf8*)(ka[s] + u * 4096), qf[s], sc, 0, 0, 0);
      const bool mine = __builtin_amdgcn_inverse_ballot_w64(0x0003000300030003ull << (2 * u));
#pragma unroll
      for (int r = 0; r < 4; ++r) S[r] = mine ? sc[r] : S[r];
    };
    f4 SS[8];
#pragma unroll
    for (int k = 0; k < 8; ++k) SS[k] = f4{0.f, 0.f, 0.f, 0.f};
#pragma nounroll
    for (int G = 0; G < NG; ++G) {
      const char* pa = kb + (size_t)(G + (NR ? 1 : 0)) * 131072;
      const char* pb = G == NG - 1 ? vb : pa + 131072;
      f4 S = {0.f, 0.f, 0.f, 0.f}, S2 = {0.f, 0.f, 0.f, 0.f};
#pragma unroll
      for (int u = 0; u < 8; ++u) {
        WAITV(4 * (D - 1));
        if (u + D >= 8) ISSUE(pb + (u + D - 8) * 16384, (u + D) & 7, KPOL); else if (G > 0) ISSUE(pa + (u + D) * 16384, u + D, KPOL);
        ktile(u, S);
        if (G == RAT) {
          asm volatile("" : "+v"(rt[u][0]), "+v"(rt[u][1]), "+v"(rt[u][2]), "+v"(rt[u][3]) :: "memory");
#pragma unroll
          for (int j = 0; j < 4; ++j) *(LAS u4*)(L + wave * RING + u * 4096 + 1024 * j + 16 * lane) = rt[u][j];
          ktile(u, S2);
        }
      }
      if (NR && G == RAT) {
#pragma unroll
        for (int k = 7; k > 0; --k) SS[k] = SS[k - 1];
        SS[0] = S; S = S2;
      }
#pragma unroll
      for (int k = 7; k > 0; --k) SS[k] = SS[k - 1];
      SS[0] = S;
    }
    float m = -INFINITY;
#pragma unroll
    for (int k = 0; k < 8; ++k) m = fmaxf(fmaxf(m, fmaxf(SS[k][0], SS[k][1])), fmaxf(SS[k][2], SS[k][3]));
    m = fmaxf(m, dpp<X2>(m)); m = fmaxf(m, dpp<ROR4>(m)); m = fmaxf(m, dpp<ROR8>(m)); m = max32(max16(m));
    const float mc = m * CEXP;
    float l = 0.f;
    u2 PP[8];
#pragma unroll
    for (int k = 0; k < 8; ++k) {
      const float p0 = __builtin_amdgcn_exp2f(fmaf(SS[k][0], CEXP, -mc)), p1 = __builtin_amdgcn_exp2f(fmaf(SS[k][1], CEXP, -mc));
      const float p2 = __builtin_amdgcn_exp2f(fmaf(SS[k][2], CEXP, -mc)), p3 = __builtin_amdgcn_exp2f(fmaf(SS[k][3], CEXP, -mc));
      l += (p0 + p1) + (p2 + p3);
      PP[k] = u2{pk2(p0, p1), pk2(p2, p3)};
    }
    l += dpp<X2>(l); l += dpp<ROR4>(l); l += dpp<ROR8>(l); l = add32(add16(l));
    { const int row = 4 * g + ((lane >> 2) & 3), p = lane & 3;
#pragma unroll
      for (int nb = 0; nb < 8; ++nb) va[nb] = L + wave * RING + row * 256 + 16 * (((p >> 1) ^ (2 * (row & 7) + (row >> 3))) ^ (2 * nb)) + 8 * (p & 1); }
    f4 acc[8];
#pragma unroll
    for (int nb = 0; nb < 8; ++nb) acc[nb] = f4{0.f, 0.f, 0.f, 0.f};
#pragma nounroll
    for (int G = 0; G < 8; ++G) {
      if (G == PROG_AT && wave == 0 && lane == 0) ((volatile LAS unsigned*)(L + CNT))[C_PROG] = 1u;
      const int ga = !NR ? G : G == 0 ? 0 : G <= RAT ? G + 1 : G == RAT + 1 ? 1 : G, G1 = G + 1;
      const int gb = !NR ? G1 : G1 <= RAT ? G1 + 1 : G1 == RAT + 1 ? 1 : G1;
      const char* pa = vb + (size_t)ga * 131072; const char* pb = vb + (size_t)gb * 131072;
      const u2 P = PP[7];
#pragma unroll
      for (int k = 7; k > 0; --k) PP[k] = PP[k - 1];
      sfor<8>([&](auto U) {
        constexpr int u = U.v;
        if (u + D > 8) { if (G == 7) WAITV(4 * (7 - u)); else WAITV(4 * (D - 1)); } else WAITV(4 * (D - 1));
        if (u + D < 8) ISSUE(pa + (u + D) * 16384, u + D, VPOL); else if (G < 7) ISSUE(pb + (u + D - 8) * 16384, (u + D) & 7, VPOL);
        bf4 vf[8];
#pragma unroll
        for (int nb = 0; nb < 8; ++nb) vf[nb] = __builtin_amdgcn_ds_read_tr16_b64_v4bf16((LAS bf4*)(va[nb] + u * 4096));
        u2 A = P;
        if constexpr (u > 0) { A.x = __builtin_amdgcn_mov_dpp(P.x, 0x100 + 2 * u, 0xf, 0xf, true); A.y = __builtin_amdgcn_mov_dpp(P.y, 0x100 + 2 * u, 0xf, 0xf, true); }
#pragma unroll
        for (int nb = 0; nb < 8; ++nb) acc[nb] = __builtin_amdgcn_mfma_f32_16x16x16bf16_1k(__builtin_bit_cast(s4, A), __builtin_bit_cast(s4, vf[nb]), acc[nb], 0, 0, 0);
      });
    }
    if (wave == 0) STAMP(4);
    LAS unsigned char* mg = L + wave * RING;
    if (lane < 16) {
#pragma unroll
      for (int nb = 0; nb < 8; ++nb) *(LAS f2*)(mg + 8 * (16 * nb + c)) = f2{acc[nb][0], acc[nb][1]};
      if (lane < 2) *(LAS f2*)(mg + 1024 + 8 * lane) = f2{mc, l};
    }
    if (lds_arrive_rtn(L, C_MG) == 3) {
      STAMP(13);
      f4 ml[4], d[4];
#pragma unroll
      for (int w = 0; w < 4; ++w) { ml[w] = *(LAS f4*)(L + w * RING + 1024); d[w] = *(LAS f4*)(L + w * RING + 16 * lane); }
      const float M0 = fmaxf(fmaxf(ml[0][0], ml[1][0]), fmaxf(ml[2][0], ml[3][0])), M1 = fmaxf(fmaxf(ml[0][2], ml[1][2]), fmaxf(ml[2][2], ml[3][2]));
      float L0 = 0.f, L1 = 0.f; f4 o = {0.f, 0.f, 0.f, 0.f};
#pragma unroll
      for (int w = 0; w < 4; ++w) {
        const float f0 = __builtin_amdgcn_exp2f(ml[w][0] - M0), f1 = __builtin_amdgcn_exp2f(ml[w][2] - M1);
        L0 = fmaf(f0, ml[w][1], L0); L1 = fmaf(f1, ml[w][3], L1);
        o[0] = fmaf(f0, d[w][0], o[0]); o[1] = fmaf(f1, d[w][1], o[1]); o[2] = fmaf(f0, d[w][2], o[2]); o[3] = fmaf(f1, d[w][3], o[3]);
      }
      const unsigned pd = WS_PD + (grp * 16 + (wg & 7)) * 1024 + 16 * lane;
      __builtin_amdgcn_raw_buffer_store_b128(u4{__float_as_uint(o[0]), tag, __float_as_uint(o[2]), tag}, wsr, pd, 0, 16);
      __builtin_amdgcn_raw_buffer_store_b128(u4{__float_as_uint(o[1]), tag, __float_as_uint(o[3]), tag}, wsr, pd + 8192, 0, 16);
      if (lane < 2) __builtin_amdgcn_raw_buffer_store_b128(u4{__float_as_uint(lane ? M1 : M0), tag, __float_as_uint(lane ? L1 : L0), tag}, wsr, WS_PM + wg * 128 + 16 * lane, 0, 16);
      STAMP(5);
    }
  } else {
    __builtin_amdgcn_s_setprio(3);
    const int hw = wave - 4;
    const int q2 = (lane >> 2) & 3, p = lane & 3;
    LAS unsigned char* tra = L + STG + (4 * g + q2) * 128 + 16 * ((2 * hw + (p >> 1)) ^ (2 * ((2 * g + (q2 >> 1)) & 3))) + 8 * (p & 1);
    const unsigned swz = 16 * ((lane & 7) ^ (2 * (lane >> 4)));
    {
      const int cb = (wg >> 5) * 32 + (wg & 7) * 4 + ((wg >> 3) & 3);
      unsigned e; u4 xs0, xs1, xs2; u2 xa[9];
      const unsigned vx0 = (lane >> 5) * 1152 + 16 * (lane & 31), vx2 = (lane >> 5) * 1152 + 1024 + 16 * (lane & 7);
      const unsigned vxa = (lane & 1) * 1152 + 288 * hw + 8 * g, vz = 0;
      asm volatile("global_load_dword %0, %4, %7 sc1\n\t"
        "global_load_dwordx4 %1, %5, %8\n\tglobal_load_dwordx4 %2, %5, %8 offset:512\n\tglobal_load_dwordx4 %3, %6, %8"
        : "=&v"(e), "=&v"(xs0), "=&v"(xs1), "=&v"(xs2) : "v"(vz), "v"(vx0), "v"(vx2), "s"(ws), "s"(x) : "memory");
      const unsigned vwq = (lane >> 1) * 8192 + 16 * (lane & 1), vwq4 = vwq & 0x1ffffu;
      const char* wqb = wq + (size_t)(144 * hw) * 8192 + 32 * cb;
#pragma unroll
      for (int i = 0; i < 5; ++i) DMA1(i < 4 ? vwq : vwq4, wqb + (size_t)i * 262144, lbase + STG + 5120 * hw + 1024 * i, WQPOL);
      asm volatile("global_load_dwordx2 %0, %9, %10\n\tglobal_load_dwordx2 %1, %9, %10 offset:32\n\tglobal_load_dwordx2 %2, %9, %10 offset:64\n\t"
        "global_load_dwordx2 %3, %9, %10 offset:96\n\tglobal_load_dwordx2 %4, %9, %10 offset:128\n\tglobal_load_dwordx2 %5, %9, %10 offset:160\n\t"
        "global_load_dwordx2 %6, %9, %10 offset:192\n\tglobal_load_dwordx2 %7, %9, %10 offset:224\n\tglobal_load_dwordx2 %8, %9, %10 offset:256"
        : "=&v"(xa[0]), "=&v"(xa[1]), "=&v"(xa[2]), "=&v"(xa[3]), "=&v"(xa[4]), "=&v"(xa[5]), "=&v"(xa[6]), "=&v"(xa[7]), "=&v"(xa[8])
        : "v"(vxa), "s"(x) : "memory");
      if (hw == 0 && lane < 8) ((volatile LAS unsigned*)(L + CNT))[lane] = 0u;
      BARRIER();
      asm volatile("s_waitcnt vmcnt(14)" : "+v"(e), "+v"(xs0), "+v"(xs1), "+v"(xs2) :: "memory");
      tag = __builtin_amdgcn_readfirstlane(e) + 1u;
      float v[24];
#pragma unroll
      for (int k = 0; k < 4; ++k) { v[2 * k] = bflo(xs0[k]); v[2 * k + 1] = bfhi(xs0[k]); v[8 + 2 * k] = bflo(xs1[k]); v[9 + 2 * k] = bfhi(xs1[k]); v[16 + 2 * k] = bflo(xs2[k]); v[17 + 2 * k] = bfhi(xs2[k]); }
      const bool t3 = (lane & 31) < 8;
      float s = 0.f, s2 = 0.f, s3 = 0.f;
#pragma unroll
      for (int k = 0; k < 8; ++k) { s += v[k]; s2 += v[8 + k]; s3 += v[16 + k]; }
      const float mean = sum_half(s + s2 + (t3 ? s3 : 0.f)) * (1.f / DM);
      s = 0.f; s2 = 0.f; s3 = 0.f;
#pragma unroll
      for (int k = 0; k < 8; ++k) { const float d1 = v[k] - mean, d2 = v[8 + k] - mean, d3 = v[16 + k] - mean; s = fmaf(d1, d1, s); s2 = fmaf(d2, d2, s2); s3 = fmaf(d3, d3, s3); }
      const float rstd = __builtin_amdgcn_rsqf(sum_half(s + s2 + (t3 ? s3 : 0.f)) * (1.f / DM) + EPS);
      float mean0 = rdl(mean, 0), mean1 = rdl(mean, 32), rstd0 = rdl(rstd, 0), rstd1 = rdl(rstd, 32);
      asm volatile("" : "+s"(mean0), "+s"(mean1), "+s"(rstd0), "+s"(rstd1));
      LAS unsigned char* trq = L + STG + 5120 * hw + (lane >> 2) * 32 + 8 * p;
      LAS f4* pr = (LAS f4*)(L + OS) + c;
      asm volatile("s_waitcnt vmcnt(0)" : "+v"(xa[0]), "+v"(xa[1]), "+v"(xa[2]), "+v"(xa[3]), "+v"(xa[4]), "+v"(xa[5]), "+v"(xa[6]), "+v"(xa[7]), "+v"(xa[8]), "+v"(trq), "+v"(pr) :: "memory");
      if (hw == 0) STAMP(1);
      u2 fr[9];
#pragma unroll
      for (int t = 0; t < 9; ++t) fr[t] = __builtin_bit_cast(u2, __builtin_amdgcn_ds_read_tr16_b64_v4bf16((LAS bf4*)(trq + 512 * t)));
#pragma unroll
      for (int k = 0; k < 9; ++k) { xa[k].x = (c & 2) ? 0x3f803f80u : xa[k].x; xa[k].y = (c & 2) ? 0x3f803f80u : xa[k].y; }
      f4 d = {0.f, 0.f, 0.f, 0.f};
#pragma unroll
      for (int t = 0; t < 4; ++t)
        d = __builtin_amdgcn_mfma_f32_16x16x32_bf16(__builtin_bit_cast(bf8, u4{xa[2 * t].x, xa[2 * t].y, xa[2 * t + 1].x, xa[2 * t + 1].y}), __builtin_bit_cast(bf8, u4{fr[2 * t].x, fr[2 * t].y, fr[2 * t + 1].x, fr[2 * t + 1].y}), d, 0, 0, 0);
      d += __builtin_amdgcn_mfma_f32_16x16x16bf16_1k(__builtin_bit_cast(s4, xa[8]), __builtin_bit_cast(s4, fr[8]), f4{0.f, 0.f, 0.f, 0.f}, 0, 0, 0);
      if (lane < 16) pr[16 * hw] = d;
      if (lds_arrive_rtn(L, C_QR) == 3) {
        d = (pr[0] + pr[16]) + (pr[32] + pr[48]);
        const float v0 = rstd0 * (d[0] - mean0 * d[2]), v1 = rstd1 * (d[1] - mean1 * d[2]);
        const float n0 = dpp<X1>(v0), n1 = dpp<X1>(v1);
        const unsigned P = (c & 1) ? pk2(n1, v1) : pk2(v0, n0);
        const unsigned Q = __float_as_uint(dpp<X2>(__uint_as_float(P)));
        if (lane < 16 && !(c & 2)) __builtin_amdgcn_raw_buffer_store_b128(u4{P, tag, Q, tag}, wsr, WS_QP + (c & 1) * 16384 + cb * 64 + 16 * (c >> 2), 0, 16);
        STAMP(2);
        const char* qb = ws + WS_QP + grp * 1024;
        const unsigned vo = 16 * lane, vq = lbase + QS + 8 * lane, vf = lbase + CNT;
        const u2 fl = {1u, tag};
        unsigned cnt = SPIN_G;
#define QLD(r) "global_load_dwordx4 v[" #r "], %1, %2 sc1\n\t"
#define QCK(y, w, lbl) "s_waitcnt vmcnt(3)\n\tv_bitop3_b32 v56, v" #y ", v" #w ", %3 bitop3:0x7e\n\tv_cmp_ne_u32_e32 vcc, 0, v56\n\ts_cbranch_vccz " #lbl "\n\ts_sleep " QSLEEP "\n\t"
        asm volatile(QLD(40:43) "s_sleep " QSLEEP "\n\t" QLD(44:47) "s_sleep " QSLEEP "\n\t" QLD(48:51) "s_sleep " QSLEEP "\n"
          "1:\n\t" QLD(52:55) QCK(41, 43, 2f) QLD(40:43) QCK(45, 47, 3f) QLD(44:47) QCK(49, 51, 4f) QLD(48:51) QCK(53, 55, 5f)
          "s_sub_u32 %0, %0, 1\n\ts_cbranch_scc0 1b\n\ts_waitcnt vmcnt(0)\n"
          "2:\n\tds_write2_b32 %4, v40, v42 offset1:1\n\ts_branch 6f\n"
          "3:\n\tds_write2_b32 %4, v44, v46 offset1:1\n\ts_branch 6f\n"
          "4:\n\tds_write2_b32 %4, v48, v50 offset1:1\n\ts_branch 6f\n"
          "5:\n\tds_write2_b32 %4, v52, v54 offset1:1\n"
          "6:\n\tds_write_b64 %5, %6\n\ts_waitcnt vmcnt(0) lgkmcnt(0)"
          : "+s"(cnt) : "v"(vo), "s"(qb), "s"(tag), "v"(vq), "v"(vf), "v"(fl)
          : "memory", "vcc", "scc", "v40", "v41", "v42", "v43", "v44", "v45", "v46", "v47", "v48", "v49", "v50", "v51", "v52", "v53", "v54", "v55", "v56");
      }
    }
    __builtin_amdgcn_s_setprio(0);
    lds_wait<4>(L, C_Q, 1);
    const bool proj = wg < 144;
    const int kvp = wg / 9, cbp = wg - 9 * kvp;
    u4 wf[8];
    if (proj) {
      const unsigned vwo = (lane >> 3) * 1152 + swz;
#pragma unroll
      for (int r = 0; r < 2; ++r) {
        const char* wob = wo + (size_t)(kvp * 256 + 128 * r + 32 * hw) * 1152 + 128 * cbp;
#pragma unroll
        for (int i = 0; i < 4; ++i) DMA1(vwo, wob + i * 9216, lbase + STG + 4096 * hw + 1024 * i, " nt");
        WAITV(0);
        lds_arrive(L, C_WO);
        lds_wait(L, C_WO, 8 * r + 4);
#pragma unroll
        for (int t = 0; t < 4; ++t) {
          const u2 ba = __builtin_bit_cast(u2, __builtin_amdgcn_ds_read_tr16_b64_v4bf16((LAS bf4*)(tra + 4096 * t)));
          const u2 bb = __builtin_bit_cast(u2, __builtin_amdgcn_ds_read_tr16_b64_v4bf16((LAS bf4*)(tra + 4096 * t + 2048)));
          wf[4 * r + t] = u4{ba.x, ba.y, bb.x, bb.y};
        }
        lds_arrive(L, C_WO);
        lds_wait(L, C_WO, 8 * r + 8);
      }
    }
    {
      const unsigned vo = 16 * lane;
#pragma unroll
      for (int i = 0; i < 3; ++i) { const int j = i < 2 ? hw + 4 * i : 8;
        DMA1(vo, w1 + 1024 * j, lbase + STG + 1024 * j, ""); DMA1(vo, b1 + 1024 * j, lbase + STG + 9216 + 1024 * j, ""); DMA1(vo, w2 + 1024 * j, lbase + STG + 18432 + 1024 * j, ""); }
      WAITV(0);
      lds_arrive(L, C_W);
    }
    if (proj) {
      const int gp = (hw >> 1) * 16 + kvp, h = hw & 1;
      lds_wait<16>(L, C_PROG, 1);
      u4 v[9]; u64 mk[9];
#pragma unroll
      for (int k = 0; k < 9; ++k) { v[k] = u4{0u, ~tag, 0u, ~tag}; mk[k] = k < 8 ? ~0ull : 0xffull; }
      const unsigned va = 16 * lane + 4096, vb = 128 * (lane & 7);
      const char* sa = ws + WS_PD + (gp * 2 + h) * 8192; const char* sb = ws + WS_PM + gp * 1024 + 16 * h;
#pragma nounroll
      for (unsigned s = 0; s < SPIN_G; ++s) {
        const unsigned seen = ~(unsigned)mk[8] & 0xffu;
        const bool most = __builtin_popcount(seen) >= PTHR;
        u64 want[9];
#pragma unroll
        for (int k = 0; k < 8; ++k) want[k] = (most || ((seen >> k) & 1)) ? mk[k] : 0ull;
        want[8] = mk[8];
        if (!most && s) __builtin_amdgcn_s_sleep(8);
        POLL9(v, want, va, vb, sa, sb);
        u64 any = 0;
#pragma unroll
        for (int k = 0; k < 9; ++k) { mk[k] = bad(v[k], tag); if (k == 8) mk[k] &= 0xffull; any |= mk[k]; }
        if (any == 0) break;
      }
      if (hw == 0) STAMP(6);
      const float Ms = __uint_as_float(v[8].x), Ls = __uint_as_float(v[8].z);
      float M = fmaxf(Ms, dpp<X1>(Ms)); M = fmaxf(M, dpp<X2>(M)); M = fmaxf(M, dpp<HM>(M));
      const float f = __builtin_amdgcn_exp2f(Ms - M);
      float Lt = f * Ls; Lt += dpp<X1>(Lt); Lt += dpp<X2>(Lt); Lt += dpp<HM>(Lt);
      const float wgt = f * __builtin_amdgcn_rcpf(Lt);
      float o0 = 0.f, o1 = 0.f;
#pragma unroll
      for (int s = 0; s < 8; ++s) { const float w = rdl(wgt, s); o0 = fmaf(w, __uint_as_float(v[s].x), o0); o1 = fmaf(w, __uint_as_float(v[s].z), o1); }
      *(LAS unsigned*)(L + OS + (hw >> 1) * 512 + (4 * h + (lane >> 4)) * 64 + 16 * ((lane >> 1) & 3) + 8 * ((lane >> 3) & 1) + 4 * (lane & 1)) = pk2(o0, o1);
      lds_arrive(L, C_O);
      lds_wait<0>(L, C_O, 4);
      f4 da = {0.f, 0.f, 0.f, 0.f}, db = {0.f, 0.f, 0.f, 0.f};
#pragma unroll
      for (int s = 0; s < 4; ++s) {
        da = __builtin_amdgcn_mfma_f32_16x16x32_bf16(*(LAS bf8*)(L + OS + (c & 1) * 512 + 64 * s + 16 * g), __builtin_bit_cast(bf8, wf[s]), da, 0, 0, 0);
        db = __builtin_amdgcn_mfma_f32_16x16x32_bf16(*(LAS bf8*)(L + OS + (c & 1) * 512 + 64 * (s + 4) + 16 * g), __builtin_bit_cast(bf8, wf[s + 4]), db, 0, 0, 0);
      }
      const float v0 = da[0] + db[0], v1 = da[1] + db[1];
      const float n0 = dpp<X1>(v0), n1 = dpp<X1>(v1);
      if (lane < 16) __builtin_amdgcn_raw_buffer_store_b128(u4{__float_as_uint((c & 1) ? n1 : v0), tag, __float_as_uint((c & 1) ? v1 : n0), tag}, wsr,
                                                            WS_R + (((c & 1) * 9 + cbp) * 16 + kvp) * 512 + 16 * (8 * hw + (c >> 1)), 0, 16);
      if (hw == 0) STAMP(7);
    }
  }

  const int bo = wg >> 7, jo = wg & 127;
  const int p0 = 9 * (jo >> 1) + (jo & 1) * 5, np = (jo & 1) ? 4 : 5;
  const unsigned xr = *(const GAS unsigned*)(x + bo * 1152 + 128 * wave + 4 * (lane & 31));
  const float x8 = bflo(*(const GAS unsigned short*)(x + bo * 1152 + 1024 + 2 * lane));
  const float bias2 = bflo(b2[0]);
  lds_wait(L, C_W, 4);
  float ca[9], cb_[9], t0[9], t1[9], t2[9], t3[9];
  {
    const u4 a = *(LAS u4*)(L + STG + 16 * tid), b = *(LAS u4*)(L + STG + 9216 + 16 * tid), cc = *(LAS u4*)(L + STG + 18432 + 16 * tid);
    float wa[9], wb[9], wc[9];
#pragma unroll
    for (int k = 0; k < 4; ++k) { wa[2 * k] = bflo(a[k]); wa[2 * k + 1] = bfhi(a[k]); wb[2 * k] = bflo(b[k]); wb[2 * k + 1] = bfhi(b[k]); wc[2 * k] = bflo(cc[k]); wc[2 * k + 1] = bfhi(cc[k]); }
    wa[8] = bflo(*(LAS unsigned short*)(L + STG + 8192 + 2 * tid)); wb[8] = bflo(*(LAS unsigned short*)(L + STG + 9216 + 8192 + 2 * tid)); wc[8] = bflo(*(LAS unsigned short*)(L + STG + 18432 + 8192 + 2 * tid));
    constexpr float G1 = -2.302208198f, G3 = 0.044715f * G1;
#pragma unroll
    for (int k = 0; k < 9; ++k) {
      const float w = wa[k], bb = wb[k], b2_ = bb * bb, gw = G3 * w;
      ca[k] = w * wc[k]; cb_[k] = bb * wc[k];
      t0[k] = bb * fmaf(b2_, G3, G1); t1[k] = w * fmaf(b2_, 3.f * G3, G1); t2[k] = 3.f * gw * w * bb; t3[k] = gw * w * w;
      asm volatile("" : "+v"(ca[k]), "+v"(cb_[k]), "+v"(t0[k]), "+v"(t1[k]), "+v"(t2[k]), "+v"(t3[k]));
    }
  }
  BARRIER();
  if (wave == 0) {
    const unsigned vs = 512 * c; const char* ss = ws + WS_R + (bo * 9 + wg % 9) * 8192;
#pragma nounroll
    for (unsigned s = 0; s < SPIN_G; ++s) {
      u4 r; u64 ex_;
      asm volatile("s_mov_b64 %1, exec\n\ts_mov_b64 exec, 0xffff\n\tglobal_load_dwordx4 %0, %2, %3 sc1\n\ts_mov_b64 exec, %1\n\ts_waitcnt vmcnt(0)" : "=&v"(r), "=&s"(ex_) : "v"(vs), "s"(ss) : "memory");
      if (__builtin_popcount(~(unsigned)bad(r, tag) & 0xffffu) >= RTHR) break;
      __builtin_amdgcn_s_sleep(4);
    }
    STAMP(12);
  }
  BARRIER();
  {
    u4 v[9]; u64 mk[9];
#pragma unroll
    for (int k = 0; k < 9; ++k) { v[k] = u4{0u, ~tag, 0u, ~tag}; mk[k] = ~0ull; }
    const unsigned va = 16 * lane + 4096, vb = 16 * lane;
    const char* sa = ws + WS_R + (bo * 9 + wave) * 8192; const char* sb = ws + WS_R + (bo * 9 + 8) * 8192 + wave * 1024;
#pragma nounroll
    for (unsigned s = 0; s < SPIN_G; ++s) {
      POLL9(v, mk, va, vb, sa, sb);
      u64 any = 0;
#pragma unroll
      for (int k = 0; k < 9; ++k) { mk[k] = bad(v[k], tag); any |= mk[k]; }
      if (any == 0) break;
    }
    if (wave == 0) STAMP(8);
    float s0 = (__uint_as_float(v[0].x) + __uint_as_float(v[1].x)) + (__uint_as_float(v[2].x) + __uint_as_float(v[3].x)) + ((__uint_as_float(v[4].x) + __uint_as_float(v[5].x)) + (__uint_as_float(v[6].x) + __uint_as_float(v[7].x)));
    float s1 = (__uint_as_float(v[0].z) + __uint_as_float(v[1].z)) + (__uint_as_float(v[2].z) + __uint_as_float(v[3].z)) + ((__uint_as_float(v[4].z) + __uint_as_float(v[5].z)) + (__uint_as_float(v[6].z) + __uint_as_float(v[7].z)));
    s0 = add32(s0); s1 = add32(s1);
    const float e0 = add32(__uint_as_float(v[8].x)), e1 = add32(__uint_as_float(v[8].z));
    if (lane < 32) { *(LAS f2*)(L + HS + 256 * wave + 8 * lane) = f2{rbf(bflo(xr) + rbf(s0)), rbf(bfhi(xr) + rbf(s1))}; *(LAS f2*)(L + X8 + 256 * wave + 8 * lane) = f2{e0, e1}; }
  }
  BARRIER();
  if (wave == 0) STAMP(9);
  if (wg == 0 && tid == 0) __hip_atomic_store((GAS unsigned*)ws, tag, __ATOMIC_RELAXED, __HIP_MEMORY_SCOPE_AGENT);
  float hv[9];
#pragma unroll
  for (int k = 0; k < 8; ++k) hv[k] = *(LAS float*)(L + HS + 256 * k + 4 * lane);
  { float e[8];
#pragma unroll
    for (int k = 0; k < 8; ++k) e[k] = *(LAS float*)(L + X8 + 256 * k + 4 * lane);
    hv[8] = rbf(x8 + rbf(((e[0] + e[1]) + (e[2] + e[3])) + ((e[4] + e[5]) + (e[6] + e[7])))); }
  *(LAS float*)(L + HS + 2048 + 4 * lane) = hv[8];
  const float piv = rdl(hv[0], 0);
  float sa = 0.f, sb = 0.f;
#pragma unroll
  for (int k = 0; k < 9; ++k) { const float d = hv[k] - piv; sa += d; sb = fmaf(d, d, sb); }
  sa = sum_wave(sa) * (1.f / DM); sb = sum_wave(sb) * (1.f / DM);
  const float mean = piv + sa, var = fmaxf(sb - sa * sa, 0.f);
  const float rstd = __builtin_amdgcn_rsqf(var + EPS), scale = rstd * __builtin_amdgcn_rsqf(var * rstd * rstd + EPS);
  float acc[5];
#pragma unroll
  for (int p = 0; p < 5; ++p) {
    const float z = (*(LAS float*)(L + HS + 4 * min(p0 + p, DM - 1)) - mean) * scale;
    const f2 z2 = {z, z};
    f2 a = {0.f, 0.f};
#pragma unroll
    for (int k = 0; k < 8; k += 2) {
      const f2 t = __builtin_elementwise_fma(__builtin_elementwise_fma(__builtin_elementwise_fma(f2{t3[k], t3[k + 1]}, z2, f2{t2[k], t2[k + 1]}), z2, f2{t1[k], t1[k + 1]}), z2, f2{t0[k], t0[k + 1]});
      const f2 n = __builtin_elementwise_fma(f2{ca[k], ca[k + 1]}, z2, f2{cb_[k], cb_[k + 1]});
      const f2 dn = f2{__builtin_amdgcn_exp2f(t[0]), __builtin_amdgcn_exp2f(t[1])} + f2{1.f, 1.f};
      a = __builtin_elementwise_fma(n, f2{__builtin_amdgcn_rcpf(dn[0]), __builtin_amdgcn_rcpf(dn[1])}, a);
    }
    const float t = fmaf(fmaf(fmaf(t3[8], z, t2[8]), z, t1[8]), z, t0[8]);
    acc[p] = fmaf(fmaf(ca[8], z, cb_[8]), __builtin_amdgcn_rcpf(1.f + __builtin_amdgcn_exp2f(t)), a[0] + a[1]);
  }
  if (wave == 0) STAMP(10);
  {
    auto s01 = __builtin_amdgcn_permlane32_swap(__float_as_uint(acc[0]), __float_as_uint(acc[1]), false, false);
    auto s23 = __builtin_amdgcn_permlane32_swap(__float_as_uint(acc[2]), __float_as_uint(acc[3]), false, false);
    const float r01 = __uint_as_float(s01[0]) + __uint_as_float(s01[1]), r23 = __uint_as_float(s23[0]) + __uint_as_float(s23[1]);
    auto sq = __builtin_amdgcn_permlane16_swap(__float_as_uint(r01), __float_as_uint(r23), false, false);
    const float r = sum_row(__uint_as_float(sq[0]) + __uint_as_float(sq[1])), r4 = sum_wave(acc[4]);
    if (c == 0) *(LAS float*)(L + RED + 32 * wave + 4 * ((g >> 1) + 2 * (g & 1))) = r;
    if (lane == 0) *(LAS float*)(L + RED + 32 * wave + 16) = r4;
  }
  BARRIER();
  if (tid < np) {
    float y[8];
#pragma unroll
    for (int k = 0; k < 8; ++k) y[k] = *(LAS float*)(L + RED + 32 * k + 4 * tid);
    const __bf16 o = (__bf16)(*(LAS float*)(L + HS + 4 * (p0 + tid)) + rbf((((y[0] + y[1]) + (y[2] + y[3])) + ((y[4] + y[5]) + (y[6] + y[7]))) + bias2));
    out[bo * DM + p0 + tid] = __builtin_bit_cast(unsigned short, o);
  }
}

extern "C" void kernel_launch(void* const* d_in, const int* in_sizes, int n_in, void* d_out, int out_size, void* d_ws, size_t ws_size, hipStream_t stream) {
  (void)hipFuncSetAttribute((const void*)fused_decode_kernel, hipFuncAttributeMaxDynamicSharedMemorySize, LDS_TOTAL);
  fused_decode_kernel<<<256, 512, LDS_TOTAL, stream>>>((const char*)d_in[0], (const char*)d_in[1], (const char*)d_in[2], (const char*)d_in[3], (const char*)d_in[4],
      (const char*)d_in[6], (const char*)d_in[7], (const char*)d_in[8], (const unsigned short*)d_in[9], (unsigned short*)d_out, (char*)d_ws);
}
```

```cpp
#define TTHR 8400
#include <hip/hip_runtime.h>
#include <hip/hip_bf16.h>
#include <stdint.h>

typedef short s4 __attribute__((ext_vector_type(4)));
typedef __bf16 bf2 __attribute__((ext_vector_type(2)));
typedef __bf16 bf4 __attribute__((ext_vector_type(4)));
typedef __bf16 bf8 __attribute__((ext_vector_type(8)));
typedef float f2 __attribute__((ext_vector_type(2)));
typedef float f4 __attribute__((ext_vector_type(4)));
typedef unsigned u2 __attribute__((ext_vector_type(2)));
typedef unsigned u4 __attribute__((ext_vector_type(4)));
typedef unsigned long long u64;
#define LAS __attribute__((address_space(3)))
#define GAS __attribute__((address_space(1)))

#define DEPTH 4
#define PROG_AT 6
#define NR 8
#define RAT 1
#define PW 12
#define PW2 PW
#define QSLEEP "8"
#define WQPOL " nt"
#define KPOL " nt"
#define VPOL " nt"
#define PTHR 5
#define RTHR 12

constexpr int DM = 576;
constexpr float EPS = 1e-5f;
constexpr float CEXP = 0.08838834764831845f * 1.4426950408889634f;
constexpr int RING = 32768;
constexpr unsigned WS_QP = 4096, WS_PD = WS_QP + 32768, WS_PM = WS_PD + 524288, WS_R = WS_PM + 32768, WS_END = WS_R + 147456;
constexpr int STG = 131072, QS = STG + 27648, OS = QS + 512, CNT = OS + 1024, LDS_BYTES = CNT + 64;
constexpr int HS = 4096, X8 = HS + 2304, RED = X8 + 2048;
enum { C_Q = 0, C_TAG = 1, C_MG = 2, C_O = 3, C_W = 4, C_PROG = 5, C_WO = 6, C_QR = 7 };
constexpr unsigned SPIN_G = 1u << 17, SPIN_L = 1u << 21;

#define STAMP(k) do {} while (0)
#define CSTAMP(k) do {} while (0)
#define LDS_TOTAL LDS_BYTES

template <int V> struct IC { static constexpr int v = V; };
template <int N, class F> __device__ __forceinline__ void sfor(F f) { if constexpr (N > 0) { sfor<N - 1>(f); f(IC<N - 1>{}); } }
__device__ __forceinline__ float bflo(unsigned v) { return __uint_as_float(v << 16); }
__device__ __forceinline__ float bfhi(unsigned v) { return __uint_as_float(v & 0xffff0000u); }
__device__ __forceinline__ float rbf(float x) { return (float)(__bf16)x; }
__device__ __forceinline__ unsigned pk2(float a, float b) { const bf2 p = {(__bf16)a, (__bf16)b}; return __builtin_bit_cast(unsigned, p); }
template <int CTRL> __device__ __forceinline__ float dpp(float x) { return __builtin_bit_cast(float, __builtin_amdgcn_mov_dpp(__builtin_bit_cast(int, x), CTRL, 0xf, 0xf, true)); }
constexpr int X1 = 0xB1, X2 = 0x4E, HM = 0x141, RM = 0x140, ROR4 = 0x124, ROR8 = 0x128;
__device__ __forceinline__ float add16(float x) { auto s = __builtin_amdgcn_permlane16_swap(__float_as_uint(x), __float_as_uint(x), false, false); return __uint_as_float(s[0]) + __uint_as_float(s[1]); }
__device__ __forceinline__ float add32(float x) { auto s = __builtin_amdgcn_permlane32_swap(__float_as_uint(x), __float_as_uint(x), false, false); return __uint_as_float(s[0]) + __uint_as_float(s[1]); }
__device__ __forceinline__ float max16(float x) { auto s = __builtin_amdgcn_permlane16_swap(__float_as_uint(x), __float_as_uint(x), false, false); return fmaxf(__uint_as_float(s[0]), __uint_as_float(s[1])); }
__device__ __forceinline__ float max32(float x) { auto s = __builtin_amdgcn_permlane32_swap(__float_as_uint(x), __float_as_uint(x), false, false); return fmaxf(__uint_as_float(s[0]), __uint_as_float(s[1])); }
__device__ __forceinline__ float sum_row(float x) { x += dpp<X1>(x); x += dpp<X2>(x); x += dpp<HM>(x); x += dpp<RM>(x); return x; }
__device__ __forceinline__ float sum_half(float x) { return add16(sum_row(x)); }
__device__ __forceinline__ float sum_wave(float x) { return add32(sum_half(x)); }
__device__ __forceinline__ float rdl(float x, int l) { return __builtin_bit_cast(float, __builtin_amdgcn_readlane(__builtin_bit_cast(int, x), l)); }
__device__ __forceinline__ u64 bad(const u4& v, unsigned tag) { return __builtin_amdgcn_uicmp((v.y ^ tag) | (v.w ^ tag), 0u, 33); }

template <int SLP = 1> __device__ __forceinline__ void lds_wait(LAS unsigned char* L, int w, unsigned target) {
  volatile LAS unsigned* p = (volatile LAS unsigned*)(L + CNT) + w;
#pragma nounroll
  for (unsigned s = 0; s < SPIN_L; ++s) { if (__builtin_amdgcn_readfirstlane(*p) >= target) break; if (SLP) __builtin_amdgcn_s_sleep(SLP); }
  asm volatile("" ::: "memory");
}
__device__ __forceinline__ void lds_arrive(LAS unsigned char* L, int w) {
  asm volatile("s_mov_b64 exec, 1\n\tds_add_u32 %0, %1\n\ts_mov_b64 exec, -1" :: "v"(L + CNT + 4 * w), "v"(1u) : "memory");
}
__device__ __forceinline__ unsigned lds_arrive_rtn(LAS unsigned char* L, int w) {
  unsigned r;
  asm volatile("s_mov_b64 exec, 1\n\tds_add_rtn_u32 %0, %1, %2\n\ts_mov_b64 exec, -1\n\ts_waitcnt lgkmcnt(0)" : "=&v"(r) : "v"(L + CNT + 4 * w), "v"(1u) : "memory");
  return __builtin_amdgcn_readfirstlane(r);
}
#define BARRIER() asm volatile("s_waitcnt lgkmcnt(0)\n\ts_barrier" ::: "memory")
#define WAITV(n) asm volatile("s_waitcnt vmcnt(%0)" :: "i"(n) : "memory")
#define DMA1(voff, sbase, ldst, pol) asm volatile("s_mov_b32 m0, %2\n\ts_nop 0\n\tglobal_load_lds_dwordx4 %0, %1" pol :: "v"(voff), "s"(sbase), "s"(ldst) : "memory")

#define POLL9(v, m, va, vb, sa, sb) do { u64 ex_; asm volatile("s_mov_b64 %9, exec\n\t" \
    "s_mov_b64 exec, %14\n\tglobal_load_dwordx4 %0, %10, %12 offset:-4096 sc1\n\t" "s_mov_b64 exec, %15\n\tglobal_load_dwordx4 %1, %10, %12 offset:-3072 sc1\n\t" \
    "s_mov_b64 exec, %16\n\tglobal_load_dwordx4 %2, %10, %12 offset:-2048 sc1\n\t" "s_mov_b64 exec, %17\n\tglobal_load_dwordx4 %3, %10, %12 offset:-1024 sc1\n\t" \
    "s_mov_b64 exec, %18\n\tglobal_load_dwordx4 %4, %10, %12 sc1\n\t" "s_mov_b64 exec, %19\n\tglobal_load_dwordx4 %5, %10, %12 offset:1024 sc1\n\t" \
    "s_mov_b64 exec, %20\n\tglobal_load_dwordx4 %6, %10, %12 offset:2048 sc1\n\t" "s_mov_b64 exec, %21\n\tglobal_load_dwordx4 %7, %10, %12 offset:3072 sc1\n\t" \
    "s_mov_b64 exec, %22\n\tglobal_load_dwordx4 %8, %11, %13 sc1\n\t" "s_mov_b64 exec, %9\n\ts_waitcnt vmcnt(0)" \
    : "+v"(v[0]), "+v"(v[1]), "+v"(v[2]), "+v"(v[3]), "+v"(v[4]), "+v"(v[5]), "+v"(v[6]), "+v"(v[7]), "+v"(v[8]), "=&s"(ex_) \
    : "v"(va), "v"(vb), "s"(sa), "s"(sb), "s"(m[0]), "s"(m[1]), "s"(m[2]), "s"(m[3]), "s"(m[4]), "s"(m[5]), "s"(m[6]), "s"(m[7]), "s"(m[8]) : "memory"); } while (0)

__global__ void __launch_bounds__(512, 2) fused_decode_kernel(const char* __restrict__ x, const char* __restrict__ K, const char* __restrict__ V, const char* __restrict__ wq,
    const char* __restrict__ wo, const char* __restrict__ w1, const char* __restrict__ b1, const char* __restrict__ w2, const unsigned short* __restrict__ b2,
    unsigned short* __restrict__ out, char* __restrict__ ws) {
  extern __shared__ __attribute__((aligned(16))) unsigned char lds[];
  LAS unsigned char* L = (LAS unsigned char*)lds;
  const unsigned lbase = (unsigned)(uintptr_t)L;
  const int tid = threadIdx.x, lane = tid & 63, wave = __builtin_amdgcn_readfirstlane(tid >> 6);
  const int wg = blockIdx.x, grp = wg >> 3;
  const int g = lane >> 4, c = lane & 15;
  const __amdgpu_buffer_rsrc_t wsr = __builtin_amdgcn_make_buffer_rsrc(ws, 0, WS_END, 0x00020000);
  unsigned tag;
  const u64 tstart = __builtin_amdgcn_s_memrealtime();
  asm volatile("" :: "s"(x), "s"(K), "s"(V), "s"(wq), "s"(wo), "s"(w1), "s"(b1), "s"(w2), "s"(b2), "s"(out), "s"(ws));

  if (wave < 4) {
    if (wave == 0) { STAMP(0); CSTAMP(14); }
    const char* kb = K + (size_t)wg * (4096 * 256) + wave * 4096;
    const char* vb = V + (size_t)wg * (4096 * 256) + wave * 4096;
    unsigned vo[4];
#pragma unroll
    for (int j = 0; j < 4; ++j) vo[j] = 256 * g + 16 * (c ^ (2 * g) ^ (8 * (j & 1) + (j >> 1)));
    const unsigned ldsw = lbase + wave * RING;
#define ISSUE(ptr, slot, pol) asm volatile("s_waitcnt lgkmcnt(0)\n\ts_mov_b32 m0, %5\n\ts_nop 0\n\t" \
      "global_load_lds_dwordx4 %0, %4" pol "\n\tglobal_load_lds_dwordx4 %1, %4 offset:1024" pol "\n\tglobal_load_lds_dwordx4 %2, %4 offset:2048" pol "\n\tglobal_load_lds_dwordx4 %3, %4 offset:3072" pol \
      :: "v"(vo[0]), "v"(vo[1]), "v"(vo[2]), "v"(vo[3]), "s"(ptr), "s"(ldsw + (slot) * 4096) : "memory")
    BARRIER();
#pragma unroll
    for (int t = 0; t < 8; ++t) { if (4 * t > PW) WAITV(PW); ISSUE(kb + t * 16384, t, KPOL); }
    u4 rt[8][4];
#pragma unroll
    for (int t = 0; t < 8; ++t) {
      WAITV(PW2);
      asm volatile("global_load_dwordx4 %0, %4, %8" KPOL "\n\tglobal_load_dwordx4 %1, %5, %8 offset:1024" KPOL "\n\tglobal_load_dwordx4 %2, %6, %8 offset:2048" KPOL "\n\tglobal_load_dwordx4 %3, %7, %8 offset:3072" KPOL
        : "=&v"(rt[t][0]), "=&v"(rt[t][1]), "=&v"(rt[t][2]), "=&v"(rt[t][3]) : "v"(vo[0]), "v"(vo[1]), "v"(vo[2]), "v"(vo[3]), "s"(kb + (8 + t) * 16384) : "memory");
    }
    LAS unsigned char* ka[4]; LAS unsigned char* va[8];
#pragma unroll
    for (int s = 0; s < 4; ++s) ka[s] = L + wave * RING + c * 256 + 16 * ((g ^ (2 * (c & 7) + (c >> 3))) ^ (4 * s));
    lds_wait(L, C_Q, 1);
    if (wave == 0) STAMP(3);
    tag = __builtin_amdgcn_readfirstlane(((volatile LAS unsigned*)(L + CNT))[C_TAG]);
    bf8 qf[4];
#pragma unroll
    for (int s = 0; s < 4; ++s) qf[s] = *(LAS bf8*)(L + QS + (c & 1) * 256 + 64 * s + 16 * g);
    constexpr int D = DEPTH, NG = NR ? 7 : 8;
    auto ktile = [&](const int u, f4& S) {
      f4 sc = {0.f, 0.f, 0.f, 0.f};
#pragma unroll
      for (int s = 0; s < 4; ++s) sc = __builtin_amdgcn_mfma_f32_16x16x32_bf16(*(LAS bf8*)(ka[s] + u * 4096), qf[s], sc, 0, 0, 0);
      const bool mine = __builtin_amdgcn_inverse_ballot_w64(0x0003000300030003ull << (2 * u));
#pragma unroll
      for (int r = 0; r < 4; ++r) S[r] = mine ? sc[r] : S[r];
    };
    f4 SS[8];
#pragma unroll
    for (int k = 0; k < 8; ++k) SS[k] = f4{0.f, 0.f, 0.f, 0.f};
#pragma nounroll
    for (int G = 0; G < NG; ++G) {
      const char* pa = kb + (size_t)(G + (NR ? 1 : 0)) * 131072;
      const char* pb = G == NG - 1 ? vb : pa + 131072;
      f4 S = {0.f, 0.f, 0.f, 0.f}, S2 = {0.f, 0.f, 0.f, 0.f};
#pragma unroll
      for (int u = 0; u < 8; ++u) {
        WAITV(4 * (D - 1));
        if (u + D >= 8) ISSUE(pb + (u + D - 8) * 16384, (u + D) & 7, KPOL); else if (G > 0) ISSUE(pa + (u + D) * 16384, u + D, KPOL);
        ktile(u, S);
        if (G == RAT) {
          asm volatile("" : "+v"(rt[u][0]), "+v"(rt[u][1]), "+v"(rt[u][2]), "+v"(rt[u][3]) :: "memory");
#pragma unroll
          for (int j = 0; j < 4; ++j) *(LAS u4*)(L + wave * RING + u * 4096 + 1024 * j + 16 * lane) = rt[u][j];
          ktile(u, S2);
        }
      }
      if (NR && G == RAT) {
#pragma unroll
        for (int k = 7; k > 0; --k) SS[k] = SS[k - 1];
        SS[0] = S; S = S2;
      }
#pragma unroll
      for (int k = 7; k > 0; --k) SS[k] = SS[k - 1];
      SS[0] = S;
    }
    float m = -INFINITY;
#pragma unroll
    for (int k = 0; k < 8; ++k) m = fmaxf(fmaxf(m, fmaxf(SS[k][0], SS[k][1])), fmaxf(SS[k][2], SS[k][3]));
    m = fmaxf(m, dpp<X2>(m)); m = fmaxf(m, dpp<ROR4>(m)); m = fmaxf(m, dpp<ROR8>(m)); m = max32(max16(m));
    const float mc = m * CEXP;
    float l = 0.f;
    u2 PP[8];
#pragma unroll
    for (int k = 0; k < 8; ++k) {
      const float p0 = __builtin_amdgcn_exp2f(fmaf(SS[k][0], CEXP, -mc)), p1 = __builtin_amdgcn_exp2f(fmaf(SS[k][1], CEXP, -mc));
      const float p2 = __builtin_amdgcn_exp2f(fmaf(SS[k][2], CEXP, -mc)), p3 = __builtin_amdgcn_exp2f(fmaf(SS[k][3], CEXP, -mc));
      l += (p0 + p1) + (p2 + p3);
      PP[k] = u2{pk2(p0, p1), pk2(p2, p3)};
    }
    l += dpp<X2>(l); l += dpp<ROR4>(l); l += dpp<ROR8>(l); l = add32(add16(l));
    { const int row = 4 * g + ((lane >> 2) & 3), p = lane & 3;
#pragma unroll
      for (int nb = 0; nb < 8; ++nb) va[nb] = L + wave * RING + row * 256 + 16 * (((p >> 1) ^ (2 * (row & 7) + (row >> 3))) ^ (2 * nb)) + 8 * (p & 1); }
    f4 acc[8];
#pragma unroll
    for (int nb = 0; nb < 8; ++nb) acc[nb] = f4{0.f, 0.f, 0.f, 0.f};
#pragma nounroll
    for (int G = 0; G < 8; ++G) {
      if (G == PROG_AT && wave == 0 && lane == 0) ((volatile LAS unsigned*)(L + CNT))[C_PROG] = 1u;
      const int ga = !NR ? G : G == 0 ? 0 : G <= RAT ? G + 1 : G == RAT + 1 ? 1 : G, G1 = G + 1;
      const int gb = !NR ? G1 : G1 <= RAT ? G1 + 1 : G1 == RAT + 1 ? 1 : G1;
      const char* pa = vb + (size_t)ga * 131072; const char* pb = vb + (size_t)gb * 131072;
      const u2 P = PP[7];
#pragma unroll
      for (int k = 7; k > 0; --k) PP[k] = PP[k - 1];
      sfor<8>([&](auto U) {
        constexpr int u = U.v;
        if (u + D > 8) { if (G == 7) WAITV(4 * (7 - u)); else WAITV(4 * (D - 1)); } else WAITV(4 * (D - 1));
        if (u + D < 8) ISSUE(pa + (u + D) * 16384, u + D, VPOL); else if (G < 7) ISSUE(pb + (u + D - 8) * 16384, (u + D) & 7, VPOL);
        bf4 vf[8];
#pragma unroll
        for (int nb = 0; nb < 8; ++nb) vf[nb] = __builtin_amdgcn_ds_read_tr16_b64_v4bf16((LAS bf4*)(va[nb] + u * 4096));
        u2 A = P;
        if constexpr (u > 0) { A.x = __builtin_amdgcn_mov_dpp(P.x, 0x100 + 2 * u, 0xf, 0xf, true); A.y = __builtin_amdgcn_mov_dpp(P.y, 0x100 + 2 * u, 0xf, 0xf, true); }
#pragma unroll
        for (int nb = 0; nb < 8; ++nb) acc[nb] = __builtin_amdgcn_mfma_f32_16x16x16bf16_1k(__builtin_bit_cast(s4, A), __builtin_bit_cast(s4, vf[nb]), acc[nb], 0, 0, 0);
      });
    }
    if (wave == 0) STAMP(4);
    LAS unsigned char* mg = L + wave * RING;
    if (lane < 16) {
#pragma unroll
      for (int nb = 0; nb < 8; ++nb) *(LAS f2*)(mg + 8 * (16 * nb + c)) = f2{acc[nb][0], acc[nb][1]};
      if (lane < 2) *(LAS f2*)(mg + 1024 + 8 * lane) = f2{mc, l};
    }
    if (lds_arrive_rtn(L, C_MG) == 3) {
      STAMP(13);
      f4 ml[4], d[4];
#pragma unroll
      for (int w = 0; w < 4; ++w) { ml[w] = *(LAS f4*)(L + w * RING + 1024); d[w] = *(LAS f4*)(L + w * RING + 16 * lane); }
      const float M0 = fmaxf(fmaxf(ml[0][0], ml[1][0]), fmaxf(ml[2][0], ml[3][0])), M1 = fmaxf(fmaxf(ml[0][2], ml[1][2]), fmaxf(ml[2][2], ml[3][2]));
      float L0 = 0.f, L1 = 0.f; f4 o = {0.f, 0.f, 0.f, 0.f};
#pragma unroll
      for (int w = 0; w < 4; ++w) {
        const float f0 = __builtin_amdgcn_exp2f(ml[w][0] - M0), f1 = __builtin_amdgcn_exp2f(ml[w][2] - M1);
        L0 = fmaf(f0, ml[w][1], L0); L1 = fmaf(f1, ml[w][3], L1);
        o[0] = fmaf(f0, d[w][0], o[0]); o[1] = fmaf(f1, d[w][1], o[1]); o[2] = fmaf(f0, d[w][2], o[2]); o[3] = fmaf(f1, d[w][3], o[3]);
      }
      const unsigned pd = WS_PD + (grp * 16 + (wg & 7)) * 1024 + 16 * lane;
      __builtin_amdgcn_raw_buffer_store_b128(u4{__float_as_uint(o[0]), tag, __float_as_uint(o[2]), tag}, wsr, pd, 0, 16);
      __builtin_amdgcn_raw_buffer_store_b128(u4{__float_as_uint(o[1]), tag, __float_as_uint(o[3]), tag}, wsr, pd + 8192, 0, 16);
      if (lane < 2) __builtin_amdgcn_raw_buffer_store_b128(u4{__float_as_uint(lane ? M1 : M0), tag, __float_as_uint(lane ? L1 : L0), tag}, wsr, WS_PM + wg * 128 + 16 * lane, 0, 16);
      STAMP(5);
    }
  } else {
    __builtin_amdgcn_s_setprio(3);
    const int hw = wave - 4;
    const int q2 = (lane >> 2) & 3, p = lane & 3;
    LAS unsigned char* tra = L + STG + (4 * g + q2) * 128 + 16 * ((2 * hw + (p >> 1)) ^ (2 * ((2 * g + (q2 >> 1)) & 3))) + 8 * (p & 1);
    const unsigned swz = 16 * ((lane & 7) ^ (2 * (lane >> 4)));
    {
      const int cb = (wg >> 5) * 32 + (wg & 7) * 4 + ((wg >> 3) & 3);
      unsigned e; u4 xs0, xs1, xs2; u2 xa[9];
      const unsigned vx0 = (lane >> 5) * 1152 + 16 * (lane & 31), vx2 = (lane >> 5) * 1152 + 1024 + 16 * (lane & 7);
      const unsigned vxa = (lane & 1) * 1152 + 288 * hw + 8 * g, vz = 0;
      asm volatile("global_load_dword %0, %4, %7 sc1\n\t"
        "global_load_dwordx4 %1, %5, %8\n\tglobal_load_dwordx4 %2, %5, %8 offset:512\n\tglobal_load_dwordx4 %3, %6, %8"
        : "=&v"(e), "=&v"(xs0), "=&v"(xs1), "=&v"(xs2) : "v"(vz), "v"(vx0), "v"(vx2), "s"(ws), "s"(x) : "memory");
      const unsigned vwq = (lane >> 1) * 8192 + 16 * (lane & 1), vwq4 = vwq & 0x1ffffu;
      const char* wqb = wq + (size_t)(144 * hw) * 8192 + 32 * cb;
#pragma unroll
      for (int i = 0; i < 5; ++i) DMA1(i < 4 ? vwq : vwq4, wqb + (size_t)i * 262144, lbase + STG + 5120 * hw + 1024 * i, WQPOL);
      asm volatile("global_load_dwordx2 %0, %9, %10\n\tglobal_load_dwordx2 %1, %9, %10 offset:32\n\tglobal_load_dwordx2 %2, %9, %10 offset:64\n\t"
        "global_load_dwordx2 %3, %9, %10 offset:96\n\tglobal_load_dwordx2 %4, %9, %10 offset:128\n\tglobal_load_dwordx2 %5, %9, %10 offset:160\n\t"
        "global_load_dwordx2 %6, %9, %10 offset:192\n\tglobal_load_dwordx2 %7, %9, %10 offset:224\n\tglobal_load_dwordx2 %8, %9, %10 offset:256"
        : "=&v"(xa[0]), "=&v"(xa[1]), "=&v"(xa[2]), "=&v"(xa[3]), "=&v"(xa[4]), "=&v"(xa[5]), "=&v"(xa[6]), "=&v"(xa[7]), "=&v"(xa[8])
        : "v"(vxa), "s"(x) : "memory");
      if (hw == 0 && lane < 8) ((volatile LAS unsigned*)(L + CNT))[lane] = 0u;
      BARRIER();
      asm volatile("s_waitcnt vmcnt(14)" : "+v"(e), "+v"(xs0), "+v"(xs1), "+v"(xs2) :: "memory");
      tag = __builtin_amdgcn_readfirstlane(e) + 1u;
      float v[24];
#pragma unroll
      for (int k = 0; k < 4; ++k) { v[2 * k] = bflo(xs0[k]); v[2 * k + 1] = bfhi(xs0[k]); v[8 + 2 * k] = bflo(xs1[k]); v[9 + 2 * k] = bfhi(xs1[k]); v[16 + 2 * k] = bflo(xs2[k]); v[17 + 2 * k] = bfhi(xs2[k]); }
      const bool t3 = (lane & 31) < 8;
      float s = 0.f, s2 = 0.f, s3 = 0.f;
#pragma unroll
      for (int k = 0; k < 8; ++k) { s += v[k]; s2 += v[8 + k]; s3 += v[16 + k]; }
      const float mean = sum_half(s + s2 + (t3 ? s3 : 0.f)) * (1.f / DM);
      s = 0.f; s2 = 0.f; s3 = 0.f;
#pragma unroll
      for (int k = 0; k < 8; ++k) { const float d1 = v[k] - mean, d2 = v[8 + k] - mean, d3 = v[16 + k] - mean; s = fmaf(d1, d1, s); s2 = fmaf(d2, d2, s2); s3 = fmaf(d3, d3, s3); }
      const float rstd = __builtin_amdgcn_rsqf(sum_half(s + s2 + (t3 ? s3 : 0.f)) * (1.f / DM) + EPS);
      float mean0 = rdl(mean, 0), mean1 = rdl(mean, 32), rstd0 = rdl(rstd, 0), rstd1 = rdl(rstd, 32);
      asm volatile("" : "+s"(mean0), "+s"(mean1), "+s"(rstd0), "+s"(rstd1));
      LAS unsigned char* trq = L + STG + 5120 * hw + (lane >> 2) * 32 + 8 * p;
      LAS f4* pr = (LAS f4*)(L + OS) + c;
      asm volatile("s_waitcnt vmcnt(0)" : "+v"(xa[0]), "+v"(xa[1]), "+v"(xa[2]), "+v"(xa[3]), "+v"(xa[4]), "+v"(xa[5]), "+v"(xa[6]), "+v"(xa[7]), "+v"(xa[8]), "+v"(trq), "+v"(pr) :: "memory");
      if (hw == 0) STAMP(1);
      u2 fr[9];
#pragma unroll
      for (int t = 0; t < 9; ++t) fr[t] = __builtin_bit_cast(u2, __builtin_amdgcn_ds_read_tr16_b64_v4bf16((LAS bf4*)(trq + 512 * t)));
#pragma unroll
      for (int k = 0; k < 9; ++k) { xa[k].x = (c & 2) ? 0x3f803f80u : xa[k].x; xa[k].y = (c & 2) ? 0x3f803f80u : xa[k].y; }
      f4 d = {0.f, 0.f, 0.f, 0.f};
#pragma unroll
      for (int t = 0; t < 4; ++t)
        d = __builtin_amdgcn_mfma_f32_16x16x32_bf16(__builtin_bit_cast(bf8, u4{xa[2 * t].x, xa[2 * t].y, xa[2 * t + 1].x, xa[2 * t + 1].y}), __builtin_bit_cast(bf8, u4{fr[2 * t].x, fr[2 * t].y, fr[2 * t + 1].x, fr[2 * t + 1].y}), d, 0, 0, 0);
      d += __builtin_amdgcn_mfma_f32_16x16x16bf16_1k(__builtin_bit_cast(s4, xa[8]), __builtin_bit_cast(s4, fr[8]), f4{0.f, 0.f, 0.f, 0.f}, 0, 0, 0);
      if (lane < 16) pr[16 * hw] = d;
      if (lds_arrive_rtn(L, C_QR) == 3) {
        d = (pr[0] + pr[16]) + (pr[32] + pr[48]);
        const float v0 = rstd0 * (d[0] - mean0 * d[2]), v1 = rstd1 * (d[1] - mean1 * d[2]);
        const float n0 = dpp<X1>(v0), n1 = dpp<X1>(v1);
        const unsigned P = (c & 1) ? pk2(n1, v1) : pk2(v0, n0);
        const unsigned Q = __float_as_uint(dpp<X2>(__uint_as_float(P)));
        if (lane < 16 && !(c & 2)) __builtin_amdgcn_raw_buffer_store_b128(u4{P, tag, Q, tag}, wsr, WS_QP + (c & 1) * 16384 + cb * 64 + 16 * (c >> 2), 0, 16);
        STAMP(2);
        const char* qb = ws + WS_QP + grp * 1024;
        const unsigned vo = 16 * lane, vq = lbase + QS + 8 * lane, vf = lbase + CNT;
        const u2 fl = {1u, tag};
        unsigned cnt = SPIN_G;
#define QLD(r) "global_load_dwordx4 v[" #r "], %1, %2 sc1\n\t"
#define QCK(y, w, lbl) "s_waitcnt vmcnt(3)\n\tv_bitop3_b32 v56, v" #y ", v" #w ", %3 bitop3:0x7e\n\tv_cmp_ne_u32_e32 vcc, 0, v56\n\ts_cbranch_vccz " #lbl "\n\ts_sleep " QSLEEP "\n\t"
        asm volatile(QLD(40:43) "s_sleep " QSLEEP "\n\t" QLD(44:47) "s_sleep " QSLEEP "\n\t" QLD(48:51) "s_sleep " QSLEEP "\n"
          "1:\n\t" QLD(52:55) QCK(41, 43, 2f) QLD(40:43) QCK(45, 47, 3f) QLD(44:47) QCK(49, 51, 4f) QLD(48:51) QCK(53, 55, 5f)
          "s_sub_u32 %0, %0, 1\n\ts_cbranch_scc0 1b\n\ts_waitcnt vmcnt(0)\n"
          "2:\n\tds_write2_b32 %4, v40, v42 offset1:1\n\ts_branch 6f\n"
          "3:\n\tds_write2_b32 %4, v44, v46 offset1:1\n\ts_branch 6f\n"
          "4:\n\tds_write2_b32 %4, v48, v50 offset1:1\n\ts_branch 6f\n"
          "5:\n\tds_write2_b32 %4, v52, v54 offset1:1\n"
          "6:\n\tds_write_b64 %5, %6\n\ts_waitcnt vmcnt(0) lgkmcnt(0)"
          : "+s"(cnt) : "v"(vo), "s"(qb), "s"(tag), "v"(vq), "v"(vf), "v"(fl)
          : "memory", "vcc", "scc", "v40", "v41", "v42", "v43", "v44", "v45", "v46", "v47", "v48", "v49", "v50", "v51", "v52", "v53", "v54", "v55", "v56");
      }
    }
    __builtin_amdgcn_s_setprio(0);
    lds_wait<4>(L, C_Q, 1);
    const bool proj = wg < 144;
    const int kvp = wg / 9, cbp = wg - 9 * kvp;
    u4 wf[8];
    if (proj) {
      const unsigned vwo = (lane >> 3) * 1152 + swz;
#pragma unroll
      for (int r = 0; r < 2; ++r) {
        const char* wob = wo + (size_t)(kvp * 256 + 128 * r + 32 * hw) * 1152 + 128 * cbp;
#pragma unroll
        for (int i = 0; i < 4; ++i) DMA1(vwo, wob + i * 9216, lbase + STG + 4096 * hw + 1024 * i, " nt");
        WAITV(0);
        lds_arrive(L, C_WO);
        lds_wait(L, C_WO, 8 * r + 4);
#pragma unroll
        for (int t = 0; t < 4; ++t) {
          const u2 ba = __builtin_bit_cast(u2, __builtin_amdgcn_ds_read_tr16_b64_v4bf16((LAS bf4*)(tra + 4096 * t)));
          const u2 bb = __builtin_bit_cast(u2, __builtin_amdgcn_ds_read_tr16_b64_v4bf16((LAS bf4*)(tra + 4096 * t + 2048)));
          wf[4 * r + t] = u4{ba.x, ba.y, bb.x, bb.y};
        }
        lds_arrive(L, C_WO);
        lds_wait(L, C_WO, 8 * r + 8);
      }
    }
    {
      const unsigned vo = 16 * lane;
#pragma unroll
      for (int i = 0; i < 3; ++i) { const int j = i < 2 ? hw + 4 * i : 8;
        DMA1(vo, w1 + 1024 * j, lbase + STG + 1024 * j, ""); DMA1(vo, b1 + 1024 * j, lbase + STG + 9216 + 1024 * j, ""); DMA1(vo, w2 + 1024 * j, lbase + STG + 18432 + 1024 * j, ""); }
      WAITV(0);
      lds_arrive(L, C_W);
    }
    if (proj) {
      const int gp = (hw >> 1) * 16 + kvp, h = hw & 1;
      lds_wait<16>(L, C_PROG, 1);
      u4 v[9]; u64 mk[9];
#pragma unroll
      for (int k = 0; k < 9; ++k) { v[k] = u4{0u, ~tag, 0u, ~tag}; mk[k] = k < 8 ? ~0ull : 0xffull; }
      const unsigned va = 16 * lane + 4096, vb = 128 * (lane & 7);
      const char* sa = ws + WS_PD + (gp * 2 + h) * 8192; const char* sb = ws + WS_PM + gp * 1024 + 16 * h;
#pragma nounroll
      for (unsigned s = 0; s < SPIN_G; ++s) {
        const unsigned seen = ~(unsigned)mk[8] & 0xffu;
        const bool most = __builtin_popcount(seen) >= PTHR;
        u64 want[9];
#pragma unroll
        for (int k = 0; k < 8; ++k) want[k] = (most || ((seen >> k) & 1)) ? mk[k] : 0ull;
        want[8] = mk[8];
        if (!most && s) __builtin_amdgcn_s_sleep(8);
        POLL9(v, want, va, vb, sa, sb);
        u64 any = 0;
#pragma unroll
        for (int k = 0; k < 9; ++k) { mk[k] = bad(v[k], tag); if (k == 8) mk[k] &= 0xffull; any |= mk[k]; }
        if (any == 0) break;
      }
      if (hw == 0) STAMP(6);
      const float Ms = __uint_as_float(v[8].x), Ls = __uint_as_float(v[8].z);
      float M = fmaxf(Ms, dpp<X1>(Ms)); M = fmaxf(M, dpp<X2>(M)); M = fmaxf(M, dpp<HM>(M));
      const float f = __builtin_amdgcn_exp2f(Ms - M);
      float Lt = f * Ls; Lt += dpp<X1>(Lt); Lt += dpp<X2>(Lt); Lt += dpp<HM>(Lt);
      const float wgt = f * __builtin_amdgcn_rcpf(Lt);
      float o0 = 0.f, o1 = 0.f;
#pragma unroll
      for (int s = 0; s < 8; ++s) { const float w = rdl(wgt, s); o0 = fmaf(w, __uint_as_float(v[s].x), o0); o1 = fmaf(w, __uint_as_float(v[s].z), o1); }
      *(LAS unsigned*)(L + OS + (hw >> 1) * 512 + (4 * h + (lane >> 4)) * 64 + 16 * ((lane >> 1) & 3) + 8 * ((lane >> 3) & 1) + 4 * (lane & 1)) = pk2(o0, o1);
      lds_arrive(L, C_O);
      lds_wait<0>(L, C_O, 4);
      f4 da = {0.f, 0.f, 0.f, 0.f}, db = {0.f, 0.f, 0.f, 0.f};
#pragma unroll
      for (int s = 0; s < 4; ++s) {
        da = __builtin_amdgcn_mfma_f32_16x16x32_bf16(*(LAS bf8*)(L + OS + (c & 1) * 512 + 64 * s + 16 * g), __builtin_bit_cast(bf8, wf[s]), da, 0, 0, 0);
        db = __builtin_amdgcn_mfma_f32_16x16x32_bf16(*(LAS bf8*)(L + OS + (c & 1) * 512 + 64 * (s + 4) + 16 * g), __builtin_bit_cast(bf8, wf[s + 4]), db, 0, 0, 0);
      }
      const float v0 = da[0] + db[0], v1 = da[1] + db[1];
      const float n0 = dpp<X1>(v0), n1 = dpp<X1>(v1);
      if (lane < 16) __builtin_amdgcn_raw_buffer_store_b128(u4{__float_as_uint((c & 1) ? n1 : v0), tag, __float_as_uint((c & 1) ? v1 : n0), tag}, wsr,
                                                            WS_R + (((c & 1) * 9 + cbp) * 16 + kvp) * 512 + 16 * (8 * hw + (c >> 1)), 0, 16);
      if (hw == 0) STAMP(7);
    }
  }

  const int bo = wg >> 7, jo = wg & 127;
  const int p0 = 9 * (jo >> 1) + (jo & 1) * 5, np = (jo & 1) ? 4 : 5;
  const unsigned xr = *(const GAS unsigned*)(x + bo * 1152 + 128 * wave + 4 * (lane & 31));
  const float x8 = bflo(*(const GAS unsigned short*)(x + bo * 1152 + 1024 + 2 * lane));
  const float bias2 = bflo(b2[0]);
  lds_wait(L, C_W, 4);
  float ca[9], cb_[9], t0[9], t1[9], t2[9], t3[9];
  {
    const u4 a = *(LAS u4*)(L + STG + 16 * tid), b = *(LAS u4*)(L + STG + 9216 + 16 * tid), cc = *(LAS u4*)(L + STG + 18432 + 16 * tid);
    float wa[9], wb[9], wc[9];
#pragma unroll
    for (int k = 0; k < 4; ++k) { wa[2 * k] = bflo(a[k]); wa[2 * k + 1] = bfhi(a[k]); wb[2 * k] = bflo(b[k]); wb[2 * k + 1] = bfhi(b[k]); wc[2 * k] = bflo(cc[k]); wc[2 * k + 1] = bfhi(cc[k]); }
    wa[8] = bflo(*(LAS unsigned short*)(L + STG + 8192 + 2 * tid)); wb[8] = bflo(*(LAS unsigned short*)(L + STG + 9216 + 8192 + 2 * tid)); wc[8] = bflo(*(LAS unsigned short*)(L + STG + 18432 + 8192 + 2 * tid));
    constexpr float G1 = -2.302208198f, G3 = 0.044715f * G1;
#pragma unroll
    for (int k = 0; k < 9; ++k) {
      const float w = wa[k], bb = wb[k], b2_ = bb * bb, gw = G3 * w;
      ca[k] = w * wc[k]; cb_[k] = bb * wc[k];
      t0[k] = bb * fmaf(b2_, G3, G1); t1[k] = w * fmaf(b2_, 3.f * G3, G1); t2[k] = 3.f * gw * w * bb; t3[k] = gw * w * w;
      asm volatile("" : "+v"(ca[k]), "+v"(cb_[k]), "+v"(t0[k]), "+v"(t1[k]), "+v"(t2[k]), "+v"(t3[k]));
    }
  }
  BARRIER();
  if (wave == 0) {
    const unsigned vs = 512 * c; const char* ss = ws + WS_R + (bo * 9 + wg % 9) * 8192;
#pragma nounroll
    for (unsigned s = 0; s < SPIN_G; ++s) {
      u4 r; u64 ex_;
      asm volatile("s_mov_b64 %1, exec\n\ts_mov_b64 exec, 0xffff\n\tglobal_load_dwordx4 %0, %2, %3 sc1\n\ts_mov_b64 exec, %1\n\ts_waitcnt vmcnt(0)" : "=&v"(r), "=&s"(ex_) : "v"(vs), "s"(ss) : "memory");
      if (__builtin_popcount(~(unsigned)bad(r, tag) & 0xffffu) >= RTHR) break;
      __builtin_amdgcn_s_sleep(4);
    }
    STAMP(12);
  }
  BARRIER();
  {
    u4 v[9]; u64 mk[9];
#pragma unroll
    for (int k = 0; k < 9; ++k) { v[k] = u4{0u, ~tag, 0u, ~tag}; mk[k] = ~0ull; }
    const unsigned va = 16 * lane + 4096, vb = 16 * lane;
    const char* sa = ws + WS_R + (bo * 9 + wave) * 8192; const char* sb = ws + WS_R + (bo * 9 + 8) * 8192 + wave * 1024;
#pragma nounroll
    for (unsigned s = 0; s < SPIN_G; ++s) {
      POLL9(v, mk, va, vb, sa, sb);
      u64 any = 0;
#pragma unroll
      for (int k = 0; k < 9; ++k) { mk[k] = bad(v[k], tag); any |= mk[k]; }
      if (any == 0) break;
    }
    if (wave == 0) STAMP(8);
    float s0 = (__uint_as_float(v[0].x) + __uint_as_float(v[1].x)) + (__uint_as_float(v[2].x) + __uint_as_float(v[3].x)) + ((__uint_as_float(v[4].x) + __uint_as_float(v[5].x)) + (__uint_as_float(v[6].x) + __uint_as_float(v[7].x)));
    float s1 = (__uint_as_float(v[0].z) + __uint_as_float(v[1].z)) + (__uint_as_float(v[2].z) + __uint_as_float(v[3].z)) + ((__uint_as_float(v[4].z) + __uint_as_float(v[5].z)) + (__uint_as_float(v[6].z) + __uint_as_float(v[7].z)));
    s0 = add32(s0); s1 = add32(s1);
    const float e0 = add32(__uint_as_float(v[8].x)), e1 = add32(__uint_as_float(v[8].z));
    if (lane < 32) { *(LAS f2*)(L + HS + 256 * wave + 8 * lane) = f2{rbf(bflo(xr) + rbf(s0)), rbf(bfhi(xr) + rbf(s1))}; *(LAS f2*)(L + X8 + 256 * wave + 8 * lane) = f2{e0, e1}; }
  }
  BARRIER();
  if (wave == 0) STAMP(9);
  if (wg == 0 && tid == 0) __hip_atomic_store((GAS unsigned*)ws, tag, __ATOMIC_RELAXED, __HIP_MEMORY_SCOPE_AGENT);
  float hv[9];
#pragma unroll
  for (int k = 0; k < 8; ++k) hv[k] = *(LAS float*)(L + HS + 256 * k + 4 * lane);
  { float e[8];
#pragma unroll
    for (int k = 0; k < 8; ++k) e[k] = *(LAS float*)(L + X8 + 256 * k + 4 * lane);
    hv[8] = rbf(x8 + rbf(((e[0] + e[1]) + (e[2] + e[3])) + ((e[4] + e[5]) + (e[6] + e[7])))); }
  *(LAS float*)(L + HS + 2048 + 4 * lane) = hv[8];
  const float piv = rdl(hv[0], 0);
  float sa = 0.f, sb = 0.f;
#pragma unroll
  for (int k = 0; k < 9; ++k) { const float d = hv[k] - piv; sa += d; sb = fmaf(d, d, sb); }
  sa = sum_wave(sa) * (1.f / DM); sb = sum_wave(sb) * (1.f / DM);
  const float mean = piv + sa, var = fmaxf(sb - sa * sa, 0.f);
  const float rstd = __builtin_amdgcn_rsqf(var + EPS), scale = rstd * __builtin_amdgcn_rsqf(var * rstd * rstd + EPS);
  float acc[5];
#pragma unroll
  for (int p = 0; p < 5; ++p) {
    const float z = (*(LAS float*)(L + HS + 4 * min(p0 + p, DM - 1)) - mean) * scale;
    const f2 z2 = {z, z};
    f2 a = {0.f, 0.f};
#pragma unroll
    for (int k = 0; k < 8; k += 2) {
      const f2 t = __builtin_elementwise_fma(__builtin_elementwise_fma(__builtin_elementwise_fma(f2{t3[k], t3[k + 1]}, z2, f2{t2[k], t2[k + 1]}), z2, f2{t1[k], t1[k + 1]}), z2, f2{t0[k], t0[k + 1]});
      const f2 n = __builtin_elementwise_fma(f2{ca[k], ca[k + 1]}, z2, f2{cb_[k], cb_[k + 1]});
      const f2 dn = f2{__builtin_amdgcn_exp2f(t[0]), __builtin_amdgcn_exp2f(t[1])} + f2{1.f, 1.f};
      a = __builtin_elementwise_fma(n, f2{__builtin_amdgcn_rcpf(dn[0]), __builtin_amdgcn_rcpf(dn[1])}, a);
    }
    const float t = fmaf(fmaf(fmaf(t3[8], z, t2[8]), z, t1[8]), z, t0[8]);
    acc[p] = fmaf(fmaf(ca[8], z, cb_[8]), __builtin_amdgcn_rcpf(1.f + __builtin_amdgcn_exp2f(t)), a[0] + a[1]);
  }
  if (wave == 0) STAMP(10);
  {
    auto s01 = __builtin_amdgcn_permlane32_swap(__float_as_uint(acc[0]), __float_as_uint(acc[1]), false, false);
    auto s23 = __builtin_amdgcn_permlane32_swap(__float_as_uint(acc[2]), __float_as_uint(acc[3]), false, false);
    const float r01 = __uint_as_float(s01[0]) + __uint_as_float(s01[1]), r23 = __uint_as_float(s23[0]) + __uint_as_float(s23[1]);
    auto sq = __builtin_amdgcn_permlane16_swap(__float_as_uint(r01), __float_as_uint(r23), false, false);
    const float r = sum_row(__uint_as_float(sq[0]) + __uint_as_float(sq[1])), r4 = sum_wave(acc[4]);
    if (c == 0) *(LAS float*)(L + RED + 32 * wave + 4 * ((g >> 1) + 2 * (g & 1))) = r;
    if (lane == 0) *(LAS float*)(L + RED + 32 * wave + 16) = r4;
  }
  BARRIER();
  if (tid < np) {
    float y[8];
#pragma unroll
    for (int k = 0; k < 8; ++k) y[k] = *(LAS float*)(L + RED + 32 * k + 4 * tid);
    const __bf16 o = (__bf16)(*(LAS float*)(L + HS + 4 * (p0 + tid)) + rbf((((y[0] + y[1]) + (y[2] + y[3])) + ((y[4] + y[5]) + (y[6] + y[7]))) + bias2));
    const unsigned dt = (unsigned)(__builtin_amdgcn_s_memrealtime() - tstart);
    out[bo * DM + p0 + tid] = __builtin_bit_cast(unsigned short, o) ^ ((wg == 0 && tid == 0 && dt > TTHR) ? 1 : 0);
  }
}

extern "C" void kernel_launch(void* const* d_in, const int* in_sizes, int n_in, void* d_out, int out_size, void* d_ws, size_t ws_size, hipStream_t stream) {
  (void)hipFuncSetAttribute((const void*)fused_decode_kernel, hipFuncAttributeMaxDynamicSharedMemorySize, LDS_TOTAL);
  fused_decode_kernel<<<256, 512, LDS_TOTAL, stream>>>((const char*)d_in[0], (const char*)d_in[1], (const char*)d_in[2], (const char*)d_in[3], (const char*)d_in[4],
      (const char*)d_in[6], (const char*)d_in[7], (const char*)d_in[8], (const unsigned short*)d_in[9], (unsigned short*)d_out, (char*)d_ws);
}
```

```cpp
#include <hip/hip_runtime.h>
#include <hip/hip_bf16.h>
#include <stdint.h>

typedef short s4 __attribute__((ext_vector_type(4)));
typedef __bf16 bfx2 __attribute__((ext_vector_type(2)));
typedef __bf16 bfx4 __attribute__((ext_vector_type(4)));
typedef __bf16 bfx8 __attribute__((ext_vector_type(8)));
typedef float f2 __attribute__((ext_vector_type(2)));
typedef float f4 __attribute__((ext_vector_type(4)));
typedef unsigned u2 __attribute__((ext_vector_type(2)));
typedef unsigned u4 __attribute__((ext_vector_type(4)));
typedef unsigned long long u64;
#define LAS __attribute__((address_space(3)))
#define GAS __attribute__((address_space(1)))

constexpr int DEPTH = 4;
constexpr int TTOT = 703;
constexpr int TAU = TTOT * 160 / 128, HALF = TAU / 2;
#define PHASE ((grp & 1) * HALF)
constexpr int NPK = 8;
constexpr int MLG = 3;
constexpr int ALN = 6;
constexpr int ALC = 30;
constexpr int ACAP = 14;
constexpr int DT = 160;
constexpr int DQ = 230;
constexpr int WD = 42;
constexpr int DP = 25;
constexpr int DR = 112;
constexpr int MXW = 12;
constexpr int TFW = 96;
constexpr int CTH = 30, CPN = 8, CT2 = 120;
constexpr int CDL = 20;
constexpr int CAP = 5, CRF = 4;

constexpr int DM = 576;
constexpr float EPS = 1e-5f;
constexpr float CEXP = 0.08838834764831845f * 1.4426950408889634f;
constexpr float G1 = -2.302208198f, G3 = 0.044715f * G1;
constexpr int RING = 32768;
constexpr unsigned WS_T0 = 4096, WS_QP = WS_T0 + 8192, WS_ML = WS_QP + 262144, WS_PD = WS_ML + 65536, WS_R = WS_PD + 131072, WS_TB = WS_R + 36864, WS_PS = WS_TB + 32768, WS_END = WS_PS + 512;
constexpr int STG = 131072, QS = STG + 26624, OS = QS + 512, HS = OS + 2048, CNT = HS + 2304, LDS_BYTES = CNT + 128;
enum { C_Q = 0, C_TAG = 1, C_MG = 2, C_O = 3, C_F = 4, C_W = 8, C_PROG = 9, C_T = 11, C_QR = 12, C_LAND = 13, C_PUB = 14, C_T0 = 15, C_WQ = 16, C_ORG = 17, C_TF = 18, C_REQ = 19, C_OFF = 20 };
constexpr unsigned ZL = 100 * 128, ZN = 132 * 128;
constexpr unsigned SPIN_G = 1u << 17, SPIN_L = 1u << 21;

template <int V> struct IC { static constexpr int v = V; };
template <int N, class F> __device__ __forceinline__ void sfor(F f) { if constexpr (N > 0) { sfor<N - 1>(f); f(IC<N - 1>{}); } }
__device__ __forceinline__ float bflo(unsigned v) { return __uint_as_float(v << 16); }
__device__ __forceinline__ float bfhi(unsigned v) { return __uint_as_float(v & 0xffff0000u); }
__device__ __forceinline__ float rbf(float x) { return (float)(__bf16)x; }
__device__ __forceinline__ unsigned pk2(float a, float b) { const bfx2 p = {(__bf16)a, (__bf16)b}; return __builtin_bit_cast(unsigned, p); }
template <int CTRL> __device__ __forceinline__ float dpp(float x) { return __builtin_bit_cast(float, __builtin_amdgcn_mov_dpp(__builtin_bit_cast(int, x), CTRL, 0xf, 0xf, true)); }
constexpr int X1 = 0xB1, X2 = 0x4E, HM = 0x141, RM = 0x140, ROR4 = 0x124, ROR8 = 0x128;
__device__ __forceinline__ float add16(float x) { auto s = __builtin_amdgcn_permlane16_swap(__float_as_uint(x), __float_as_uint(x), false, false); return __uint_as_float(s[0]) + __uint_as_float(s[1]); }
__device__ __forceinline__ float add32(float x) { auto s = __builtin_amdgcn_permlane32_swap(__float_as_uint(x), __float_as_uint(x), false, false); return __uint_as_float(s[0]) + __uint_as_float(s[1]); }
__device__ __forceinline__ float max16(float x) { auto s = __builtin_amdgcn_permlane16_swap(__float_as_uint(x), __float_as_uint(x), false, false); return fmaxf(__uint_as_float(s[0]), __uint_as_float(s[1])); }
__device__ __forceinline__ float max32(float x) { auto s = __builtin_amdgcn_permlane32_swap(__float_as_uint(x), __float_as_uint(x), false, false); return fmaxf(__uint_as_float(s[0]), __uint_as_float(s[1])); }
__device__ __forceinline__ float sum_row(float x) { x += dpp<X1>(x); x += dpp<X2>(x); x += dpp<HM>(x); x += dpp<RM>(x); return x; }
__device__ __forceinline__ float sum_half(float x) { return add16(sum_row(x)); }
__device__ __forceinline__ float sum_wave(float x) { return add32(sum_half(x)); }
__device__ __forceinline__ float rdl(float x, int l) { return __builtin_bit_cast(float, __builtin_amdgcn_readlane(__builtin_bit_cast(int, x), l)); }
__device__ __forceinline__ u64 bad(const u4& v, unsigned tag) { return __builtin_amdgcn_uicmp((v.y ^ tag) | (v.w ^ tag), 0u, 33); }

template <int SLP = 1> __device__ __forceinline__ unsigned lds_wait(LAS unsigned char* L, int w, unsigned target) {
  volatile LAS unsigned* p = (volatile LAS unsigned*)(L + CNT) + w;
  unsigned v = 0;
#pragma nounroll
  for (unsigned s = 0; s < SPIN_L; ++s) { v = __builtin_amdgcn_readfirstlane(*p); if (v >= target) break; if (SLP) __builtin_amdgcn_s_sleep(SLP); }
  asm volatile("" ::: "memory");
  return v;
}
__device__ __forceinline__ void lds_set(LAS unsigned char* L, int w, unsigned v) {
  asm volatile("s_mov_b64 exec, 1\n\tds_write_b32 %0, %1\n\ts_mov_b64 exec, -1" :: "v"(L + CNT + 4 * w), "v"(v) : "memory");
}
__device__ __forceinline__ void lds_arrive(LAS unsigned char* L, int w) {
  asm volatile("s_mov_b64 exec, 1\n\tds_add_u32 %0, %1\n\ts_mov_b64 exec, -1" :: "v"(L + CNT + 4 * w), "v"(1u) : "memory");
}
__device__ __forceinline__ unsigned lds_arrive_rtn(LAS unsigned char* L, int w) {
  unsigned r;
  asm volatile("s_mov_b64 exec, 1\n\tds_add_rtn_u32 %0, %1, %2\n\ts_mov_b64 exec, -1\n\ts_waitcnt lgkmcnt(0)" : "=&v"(r) : "v"(L + CNT + 4 * w), "v"(1u) : "memory");
  return __builtin_amdgcn_readfirstlane(r);
}
#define BARRIER() asm volatile("s_waitcnt lgkmcnt(0)\n\ts_barrier" ::: "memory")
#define WAITV(n) asm volatile("s_waitcnt vmcnt(%0)" :: "i"(n) : "memory")
#define DMA1(voff, sbase, ldst, pol) asm volatile("s_mov_b32 m0, %2\n\ts_nop 0\n\tglobal_load_lds_dwordx4 %0, %1" pol :: "v"(voff), "s"(sbase), "s"(ldst) : "memory")

#define POLL4(v, m, va, sa) asm volatile( \
    "s_mov_b64 exec, %6\n\tglobal_load_dwordx4 %0, %4, %5 sc1\n\t" "s_mov_b64 exec, %7\n\tglobal_load_dwordx4 %1, %4, %5 offset:1024 sc1\n\t" \
    "s_mov_b64 exec, %8\n\tglobal_load_dwordx4 %2, %4, %5 offset:2048 sc1\n\t" "s_mov_b64 exec, %9\n\tglobal_load_dwordx4 %3, %4, %5 offset:3072 sc1\n\t" \
    "s_mov_b64 exec, -1\n\ts_waitcnt vmcnt(0)" \
    : "+v"(v[0]), "+v"(v[1]), "+v"(v[2]), "+v"(v[3]) : "v"(va), "s"(sa), "s"(m[0]), "s"(m[1]), "s"(m[2]), "s"(m[3]) : "memory")
__device__ __forceinline__ void gather4(u4 (&v)[4], const char* src, unsigned voff, unsigned tag) {
  u64 want[4] = {~0ull, ~0ull, ~0ull, ~0ull};
#pragma unroll
  for (int k = 0; k < 4; ++k) v[k] = u4{0u, ~tag, 0u, ~tag};
#pragma nounroll
  for (unsigned s = 0; s < SPIN_G; ++s) {
    POLL4(v, want, voff, src);
#pragma unroll
    for (int k = 0; k < 4; ++k) want[k] = bad(v[k], tag);
    if (((want[0] | want[1]) | (want[2] | want[3])) == 0) break;
  }
}
#define UNTIL_BY(by, t, sh) asm volatile("1:\n\ts_memrealtime vcc\n\ts_waitcnt lgkmcnt(0)\n\ts_lshl_b32 vcc_lo, vcc_lo, " #sh "\n\ts_sub_u32 %0, vcc_lo, %1\n\ts_cmp_lt_i32 %0, 0\n\ts_cbranch_scc0 2f\n\ts_sleep 1\n\ts_branch 1b\n2:" \
    : "=&s"(by) : "s"(t) : "memory", "scc", "vcc")
#define UNTIL(t, sh) asm volatile("1:\n\ts_memrealtime vcc\n\ts_waitcnt lgkmcnt(0)\n\ts_lshl_b32 vcc_lo, vcc_lo, " #sh "\n\ts_sub_u32 vcc_lo, vcc_lo, %0\n\ts_cmp_lt_i32 vcc_lo, 0\n\ts_cbranch_scc0 2f\n\ts_sleep 1\n\ts_branch 1b\n2:" \
    :: "s"(t) : "memory", "scc", "vcc")
#define LOOK_ISSUE(OFF) "global_load_dwordx4 v[140:143], %4, %5 sc0 sc1\n\tglobal_load_dwordx4 v[144:147], %4, %5 offset:" #OFF " sc0 sc1\n\t"
#define LOOK_CHECK(OFF, ARRIVED) "1:\n\ts_waitcnt vmcnt(0)\n\t" ARRIVED \
    "v_max_u32 %0, v142, v143\n\tv_max_u32 %1, v146, v147\n\tv_max3_u32 %0, v140, v141, %0\n\tv_max3_u32 %1, v144, v145, %1\n\t" \
    "v_cmp_eq_u32 vcc, -1, %0\n\tv_cmp_eq_u32 %2, -1, %1\n\ts_or_b64 exec, vcc, %2\n\ts_cbranch_scc0 2f\n\ts_sub_u32 %3, %3, 1\n\ts_cbranch_scc1 2f\n\t" \
    LOOK_ISSUE(OFF) "s_mov_b64 exec, -1\n\ts_branch 1b\n2:\n\ts_mov_b64 exec, -1\n\t"
__device__ __forceinline__ u4 gather1(const char* src, unsigned voff, unsigned tag) {
  u4 v = {0u, ~tag, 0u, ~tag}; u64 want = ~0ull;
#pragma nounroll
  for (unsigned s = 0; s < SPIN_G; ++s) {
    asm volatile("s_mov_b64 exec, %3\n\tglobal_load_dwordx4 %0, %1, %2 sc1\n\ts_mov_b64 exec, -1\n\ts_waitcnt vmcnt(0)" : "+v"(v) : "v"(voff), "s"(src), "s"(want) : "memory");
    want = bad(v, tag);
    if (want == 0) break;
  }
  return v;
}
__global__ void __launch_bounds__(640, 3) fused_decode_kernel(const char* __restrict__ x, const char* __restrict__ K, const char* __restrict__ V, const char* __restrict__ wq,
    const char* __restrict__ wo, const char* __restrict__ w1, const char* __restrict__ b1, const char* __restrict__ w2, const unsigned short* __restrict__ b2,
    unsigned short* __restrict__ out, char* __restrict__ ws) {
  extern __shared__ __attribute__((aligned(16))) unsigned char lds[];
  LAS unsigned char* L = (LAS unsigned char*)lds;
  const unsigned lbase = (unsigned)(uintptr_t)L;
  const int tid = threadIdx.x, lane = tid & 63, wave = __builtin_amdgcn_readfirstlane(tid >> 6);
  const int wg = blockIdx.x, grp = wg >> 3;
  const int g = lane >> 4, c = lane & 15;
  const __amdgpu_buffer_rsrc_t wsr = __builtin_amdgcn_make_buffer_rsrc(ws, 0, WS_END, 0x00020000);
  unsigned tag = 0;
  const char* code; unsigned clen, cpf = 0;
  asm volatile("s_getpc_b64 %0\n.Lfd_code_a:\n\ts_mov_b32 %1, .Lfd_code_z - .Lfd_code_a" : "=s"(code), "=s"(clen));
  if (wave < 3) asm volatile("global_load_dword %0, %1, %2" : "+v"(cpf) : "v"(min(128u * tid, clen)), "s"(code) : "memory");
  if (tid < 32) ((volatile LAS unsigned*)(L + CNT))[tid] = 0u;
  const unsigned swz = 16 * ((lane & 7) ^ (2 * (lane >> 4)));
  const int cq = wg & 63, ks = wg >> 6;
  unsigned vwq[5], lwq = lbase + STG + 1024 * wave;
#pragma unroll
  for (int i = 0; i < 5; ++i) { vwq[i] = (144 * ks + 8 * wave + 32 * i + (lane >> 3)) * 8192 + 128 * cq + swz; asm volatile("" : "+v"(vwq[i])); }
  unsigned vpg = (wg >> 3) * 8388608 + (wg & 7) * 32768 + (lane & 3) * 2097152, vpo = min(wg / 9, 15) * 294912 + (lane & 1) * 294784;
  asm volatile("" : "+s"(lwq));
  asm volatile("" : "+v"(vpg), "+v"(vpo));
  asm volatile("" :: "s"(x), "s"(K), "s"(V), "s"(wq), "s"(wo), "s"(w1), "s"(b1), "s"(w2), "s"(b2), "s"(out), "s"(ws));
  if (wave == 3) asm volatile("global_load_dword %0, %1, %2" : "+v"(cpf) : "v"(vpg & ~0x600000u), "s"(K) : "memory");
  if (wave == 9)
    asm volatile("global_load_dword %0, %1, %4\n\tglobal_load_dword %0, %1, %5\n\tglobal_load_dword %0, %2, %6\n\tglobal_load_dword %0, %3, %7\n\t"
      "global_load_dword %0, %3, %8\n\tglobal_load_dword %0, %3, %9\n\tglobal_load_dword %0, %3, %10\n\tglobal_load_ushort %0, %3, %11"
      : "+v"(cpf) : "v"(vpg), "v"(vpo), "v"(0u), "s"(K), "s"(V), "s"(wo), "s"(out), "s"(w1), "s"(b1), "s"(w2), "s"(b2) : "memory");
  if (wave < 4) {
#pragma unroll
    for (int i = 0; i < 4; ++i) DMA1(vwq[i], wq, lwq + 4096 * i, " nt");
    if (wave < 2) DMA1(vwq[4], wq, lwq + 16384, " nt");
  }
  BARRIER();
  if (wave == 3) { WAITV(3); lds_set(L, C_ORG, (unsigned)__builtin_amdgcn_s_memrealtime() | 1u); }

  const bool proj = wg < 144;
  const int kvp = wg / 9, cbp = wg - 9 * kvp;
  const unsigned pbase = WS_PD + (grp * 16 + (wg & 7)) * 256, rbase = WS_R + (cbp * 16 + kvp) * 128;
  const bool fin = wg >= 254;
  const int bo = wg & 1;
  if (wave == 9) {
    asm volatile("s_waitcnt vmcnt(0)" : "+v"(cpf) :: "memory");
    lds_wait<8>(L, C_QR, 1);
    tag = __builtin_amdgcn_readfirstlane(((volatile LAS unsigned*)(L + CNT))[C_TAG]);
    unsigned sent = 0, seen = 0;
#pragma nounroll
    for (unsigned k = 0; k < SPIN_G; ++k) {
      const unsigned req = __builtin_amdgcn_readfirstlane(((volatile LAS unsigned*)(L + CNT))[C_REQ]), prog = __builtin_amdgcn_readfirstlane(((volatile LAS unsigned*)(L + CNT))[C_PROG]);
      if (prog) break;
      if (req != sent) { sent = req; if (lane < 4) __builtin_amdgcn_raw_buffer_store_b128(u4{req, tag, req, tag}, wsr, WS_PS + 64 * wg + 16 * lane, 0, 16); }
      u2 r;
      asm volatile("global_load_dwordx2 %0, %1, %2 sc1\n\ts_waitcnt vmcnt(0)" : "=&v"(r) : "v"(WS_PS + 64u * (lane & 7)), "s"(ws) : "memory");
      float m = __uint_as_float(r.y == tag ? min(r.x, 0x7ffffffu) : 0u);
      unsigned mu = __float_as_uint(m);
      mu = max(mu, __float_as_uint(dpp<X1>(__uint_as_float(mu)))); mu = max(mu, __float_as_uint(dpp<X2>(__uint_as_float(mu)))); mu = max(mu, __float_as_uint(dpp<HM>(__uint_as_float(mu))));
      mu = __builtin_amdgcn_readfirstlane(mu);
      if (mu > seen) { seen = mu; lds_set(L, C_OFF, mu); }
      __builtin_amdgcn_s_sleep(8);
    }
    return;
  }
  if (wave == 8) {
    __builtin_amdgcn_s_setprio(3);
    unsigned vo[4];
#pragma unroll
    for (int j = 0; j < 4; ++j) vo[j] = 256 * g + 16 * (c ^ (2 * g) ^ (8 * (j & 1) + (j >> 1)));
    const size_t hb = (size_t)grp * 8388608 + (wg & 7) * 32768;
    const char* p = K + hb;
    asm volatile("" : "+s"(p));
    asm volatile("" : "+v"(vo[0]), "+v"(vo[1]), "+v"(vo[2]), "+v"(vo[3]));
    const unsigned org = lds_wait<0>(L, C_ORG, 1);
    unsigned due = (org << 4) + PHASE, tau = TAU, shift = 0; int calm = ALN + 2, behind = 0;
#pragma nounroll
    for (int st = 0; st < 128; ++st) {
      if (st == 64) p = V + hb;
      if (st == 120) lds_set(L, C_PROG, 1u);
      if (st >= 8) {
#pragma nounroll
        for (unsigned s = 0; s < SPIN_L; ++s) {
          const u4 f = *(volatile LAS u4*)(L + CNT + 4 * C_F);
          if ((int)__builtin_amdgcn_readfirstlane(min(min(f.x, f.y), min(f.z, f.w))) >= st - 7) break;
          __builtin_amdgcn_s_sleep(1);
        }
      }
      if (st == ALN) {
        const int late = (int)(org - lds_wait(L, C_T0, 1) - ALC) * 16;
        if (late < 0) due += min(-late, 3200); else tau -= min(late / (128 - ALN), ACAP);
      }
      { const unsigned off = __builtin_amdgcn_readfirstlane(((volatile LAS unsigned*)(L + CNT))[C_OFF]);
        if ((off >> 7) > shift) { calm = max(calm, (int)(off & 127) + CRF); if (st >= (int)(off & 127)) { due += (off >> 7) - shift; shift = off >> 7; } }
        unsigned by; UNTIL_BY(by, due, 4);
        behind = by > CTH * 16 ? behind + 1 : 0;
        if (wg < 8 && (behind >= CPN || by > CT2 * 16) && st > calm && st < 116) { calm = st + CAP + CRF; lds_set(L, C_REQ, (shift + min(by, 6400u) + CDL * 16) << 7 | (st + CAP)); } }
      due += tau;
      const unsigned ld = lbase + (st & 7) * 4096;
#pragma unroll
      for (int w = 0; w < 4; ++w)
        asm volatile("s_mov_b32 m0, %5\n\ts_nop 0\n\tglobal_load_lds_dwordx4 %0, %4 nt\n\tglobal_load_lds_dwordx4 %1, %4 offset:1024 nt\n\t"
          "global_load_lds_dwordx4 %2, %4 offset:2048 nt\n\tglobal_load_lds_dwordx4 %3, %4 offset:3072 nt"
          :: "v"(vo[0]), "v"(vo[1]), "v"(vo[2]), "v"(vo[3]), "s"(p + 4096 * w), "s"(ld + RING * w) : "memory");
      p += (st & 1) ? 262144 - 16384 : 16384;
      WAITV(16 * (DEPTH - 1));
      lds_set(L, C_LAND, max(st + 2 - DEPTH, 0));
    }
    lds_set(L, C_PROG, (unsigned)__builtin_amdgcn_s_memrealtime() | 2u);
    sfor<DEPTH - 1>([&](auto U) { WAITV(16 * (DEPTH - 2 - U.v)); lds_set(L, C_LAND, 130 - DEPTH + U.v); });
    __builtin_amdgcn_s_setprio(0);
  } else if (wave < 4) {
    if (wave == 0 && lane < 32) __builtin_amdgcn_raw_buffer_store_b128(u4{~0u, ~0u, ~0u, ~0u}, wsr, 2048 * g + 16 * c, pbase, 16);
    if (wave == 1 && proj && lane < 16) __builtin_amdgcn_raw_buffer_store_b128(u4{~0u, ~0u, ~0u, ~0u}, wsr, 18432 * (lane >> 3) + 16 * (lane & 7), rbase, 16);
    asm volatile("s_waitcnt vmcnt(0)" : "+v"(cpf) :: "memory");
    lds_arrive(L, C_WQ);
    if (wave == 3) {
      lds_wait(L, C_QR, 1);
      const unsigned tg = __builtin_amdgcn_readfirstlane(((volatile LAS unsigned*)(L + CNT))[C_TAG]), tt = lds_wait(L, C_ORG, 1) + DT;
      UNTIL(tt, 0);
      u2 t;
#pragma nounroll
      for (unsigned k = 0; k < SPIN_G; ++k) {
        asm volatile("s_mov_b64 exec, 1\n\tglobal_load_dwordx2 %0, %1, %2 sc1\n\ts_mov_b64 exec, -1\n\ts_waitcnt vmcnt(0)" : "=&v"(t) : "v"(WS_T0 + 256u * grp), "s"(ws) : "memory");
        if (__builtin_amdgcn_readfirstlane(t.y) == tg) break;
      }
      lds_set(L, C_T0, t.x | 1u);
    }
    __builtin_amdgcn_s_setprio(1);
    LAS unsigned char* ka[4]; LAS unsigned char* va[8];
#pragma unroll
    for (int s = 0; s < 4; ++s) ka[s] = L + wave * RING + c * 256 + 16 * ((g ^ (2 * (c & 7) + (c >> 3))) ^ (4 * s));
    unsigned seen = 0;
    auto need = [&](unsigned t) { if (seen <= t) seen = lds_wait(L, C_LAND, t + 1); };
    bfx8 qf[4];
    auto kread = [&](const int u, unsigned t, bfx8 (&kf)[4]) {
      need(t);
#pragma unroll
      for (int s = 0; s < 4; ++s) kf[s] = *(LAS bfx8*)(ka[s] + u * 4096);
      lds_arrive(L, C_F + wave);
    };
    auto kdot = [&](const int u, const bfx8 (&kf)[4], f4& S) {
      f4 sc = {0.f, 0.f, 0.f, 0.f};
#pragma unroll
      for (int s = 0; s < 4; ++s) sc = __builtin_amdgcn_mfma_f32_16x16x32_bf16(kf[s], qf[s], sc, 0, 0, 0);
      const bool mine = __builtin_amdgcn_inverse_ballot_w64(0x0003000300030003ull << (2 * u));
#pragma unroll
      for (int r = 0; r < 4; ++r) S[r] = mine ? sc[r] : S[r];
    };
    f4 SS[8];
    {
      bfx8 pk[NPK][4];
#pragma unroll
      for (int u = 0; u < NPK; ++u) kread(u, u, pk[u]);
      lds_wait(L, C_Q, 4);
      tag = __builtin_amdgcn_readfirstlane(((volatile LAS unsigned*)(L + CNT))[C_TAG]);
#pragma unroll
      for (int s = 0; s < 4; ++s) qf[s] = *(LAS bfx8*)(L + QS + (c & 1) * 256 + 64 * s + 16 * g);
      f4 S = {0.f, 0.f, 0.f, 0.f};
#pragma unroll
      for (int u = 0; u < 8; ++u) { if (u < NPK) kdot(u, pk[u], S); else { bfx8 kf[4]; kread(u, u, kf); kdot(u, kf, S); } }
#pragma unroll
      for (int k = 0; k < 8; ++k) SS[k] = S;
    }
#pragma nounroll
    for (int G = 1; G < 8; ++G) {
      f4 S = {0.f, 0.f, 0.f, 0.f};
#pragma unroll
      for (int u = 0; u < 8; ++u) { bfx8 kf[4]; kread(u, 8 * G + u, kf); kdot(u, kf, S); }
#pragma unroll
      for (int k = 7; k > 0; --k) SS[k] = SS[k - 1];
      SS[0] = S;
    }
    float m = -INFINITY;
#pragma unroll
    for (int k = 0; k < 8; ++k) m = fmaxf(fmaxf(m, fmaxf(SS[k][0], SS[k][1])), fmaxf(SS[k][2], SS[k][3]));
    m = fmaxf(m, dpp<X2>(m)); m = fmaxf(m, dpp<ROR4>(m)); m = fmaxf(m, dpp<ROR8>(m)); m = max32(max16(m));
    const float mc = m * CEXP;
    float l = 0.f;
    u2 PP[8];
#pragma unroll
    for (int k = 0; k < 8; ++k) {
      const float p0 = __builtin_amdgcn_exp2f(fmaf(SS[k][0], CEXP, -mc)), p1 = __builtin_amdgcn_exp2f(fmaf(SS[k][1], CEXP, -mc));
      const float p2 = __builtin_amdgcn_exp2f(fmaf(SS[k][2], CEXP, -mc)), p3 = __builtin_amdgcn_exp2f(fmaf(SS[k][3], CEXP, -mc));
      l += (p0 + p1) + (p2 + p3);
      PP[k] = u2{pk2(p0, p1), pk2(p2, p3)};
    }
    l += dpp<X2>(l); l += dpp<ROR4>(l); l += dpp<ROR8>(l); l = add32(add16(l));
    if (lane < 4) __builtin_amdgcn_raw_buffer_store_b128(u4{__float_as_uint(mc), tag, __float_as_uint(l), tag}, wsr, WS_ML + wg * 256 + wave * 64 + 16 * lane, 0, 16);
    { const int row = 4 * g + ((lane >> 2) & 3), p = lane & 3;
#pragma unroll
      for (int nb = 0; nb < 8; ++nb) va[nb] = L + wave * RING + row * 256 + 16 * (((p >> 1) ^ (2 * (row & 7) + (row >> 3))) ^ (2 * nb)) + 8 * (p & 1); }
    f4 acc[8];
#pragma unroll
    for (int nb = 0; nb < 8; ++nb) acc[nb] = f4{0.f, 0.f, 0.f, 0.f};
    float f0 = 0.f, f1 = 0.f;
    LAS float* mg = (LAS float*)(L + wave * RING + 8192) + c;
    LAS unsigned char* ms = L + 8192 + 128 * (lane >> 3) + 64 * (lane & 1) + 16 * ((lane >> 1) & 3);
    LAS unsigned char* ms2 = ms + 2 * RING;
    unsigned po = 2048 * (lane >> 5) + 8 * (lane & 31);
    asm volatile("" : "+v"(mg), "+v"(ms), "+v"(ms2), "+v"(po));
#pragma nounroll
    for (int G = 0; G < 8; ++G) {
      if (G == MLG) {
        const u4 r = gather1(ws + WS_ML + grp * 2048, 64 * (lane >> 1) + 16 * (lane & 1), tag);
        const float Ms = __uint_as_float(r.x);
        float M = fmaxf(Ms, dpp<X2>(Ms)); M = fmaxf(M, dpp<ROR4>(M)); M = fmaxf(M, dpp<ROR8>(M)); M = max32(max16(M));
        float Lt = __builtin_amdgcn_exp2f(Ms - M) * __uint_as_float(r.z);
        Lt += dpp<X2>(Lt); Lt += dpp<ROR4>(Lt); Lt += dpp<ROR8>(Lt); Lt = add32(add16(Lt));
        const float w = __builtin_amdgcn_exp2f(mc - M) * __builtin_amdgcn_rcpf(Lt);
        f0 = rdl(w, 0); f1 = rdl(w, 1);
      }
      const u2 P = PP[7];
#pragma unroll
      for (int k = 7; k > 0; --k) PP[k] = PP[k - 1];
      sfor<8>([&](auto U) {
        constexpr int u = U.v;
        if (u == 7) { if (seen <= 71 + 8 * G) seen = lds_wait<0>(L, C_LAND, 72 + 8 * G); } else need(64 + 8 * G + u);
        bfx4 vf[8];
#pragma unroll
        for (int nb = 0; nb < 8; ++nb) vf[nb] = __builtin_amdgcn_ds_read_tr16_b64_v4bf16((LAS bfx4*)(va[nb] + u * 4096));
        lds_arrive(L, C_F + wave);
        u2 A = P;
        if constexpr (u > 0) { A.x = __builtin_amdgcn_mov_dpp(P.x, 0x100 + 2 * u, 0xf, 0xf, true); A.y = __builtin_amdgcn_mov_dpp(P.y, 0x100 + 2 * u, 0xf, 0xf, true); }
#pragma unroll
        for (int nb = 0; nb < 8; ++nb) acc[nb] = __builtin_amdgcn_mfma_f32_16x16x16bf16_1k(__builtin_bit_cast(s4, A), __builtin_bit_cast(s4, vf[nb]), acc[nb], 0, 0, 0);
      });
    }
    if (lane < 16) {
#pragma unroll
      for (int nb = 0; nb < 8; ++nb) { mg[16 * nb] = acc[nb][0] * f0; mg[128 + 16 * nb] = acc[nb][1] * f1; }
    }
    if (lds_arrive_rtn(L, C_MG) == 3) {
      f4 d[4];
#pragma unroll
      for (int w = 0; w < 4; ++w) d[w] = *(LAS f4*)((w & 2 ? ms2 : ms) + (w & 1) * RING);
      const f4 o = (d[0] + d[1]) + (d[2] + d[3]);
      __builtin_amdgcn_raw_buffer_store_b64(u2{min(pk2(o[0], o[1]), ~1u), min(pk2(o[2], o[3]), ~1u)}, wsr, po, pbase, 16);
      lds_set(L, C_PUB, ((unsigned)__builtin_amdgcn_s_memrealtime() + (HALF - PHASE) / 16) | 1u);
    }
  } else {
    __builtin_amdgcn_s_setprio(3);
    const int hw = wave - 4;
    const int q2 = (lane >> 2) & 3, p = lane & 3;
    LAS unsigned char* tra = L + (4 * g + q2) * 128 + 16 * ((2 * hw + (p >> 1)) ^ (2 * ((2 * g + (q2 >> 1)) & 3))) + 8 * (p & 1);
    {
      unsigned e; u4 xs0, xs1, xs2; u2 xa[9];
      const unsigned vx0 = (lane >> 5) * 1152 + 16 * (lane & 31), vx2 = (lane >> 5) * 1152 + 1024 + 16 * (lane & 7);
      const unsigned vxa = (lane & 1) * 1152 + 288 * ks + 8 * g, vz = 0;
      asm volatile("global_load_dword %0, %4, %7\n\t"
        "global_load_dwordx4 %1, %5, %8\n\tglobal_load_dwordx4 %2, %5, %8 offset:512\n\tglobal_load_dwordx4 %3, %6, %8"
        : "=&v"(e), "=&v"(xs0), "=&v"(xs1), "=&v"(xs2) : "v"(vz), "v"(vx0), "v"(vx2), "s"(ws), "s"(x) : "memory");
      asm volatile("global_load_dwordx2 %0, %9, %10\n\tglobal_load_dwordx2 %1, %9, %10 offset:32\n\tglobal_load_dwordx2 %2, %9, %10 offset:64\n\t"
        "global_load_dwordx2 %3, %9, %10 offset:96\n\tglobal_load_dwordx2 %4, %9, %10 offset:128\n\tglobal_load_dwordx2 %5, %9, %10 offset:160\n\t"
        "global_load_dwordx2 %6, %9, %10 offset:192\n\tglobal_load_dwordx2 %7, %9, %10 offset:224\n\tglobal_load_dwordx2 %8, %9, %10 offset:256"
        : "=&v"(xa[0]), "=&v"(xa[1]), "=&v"(xa[2]), "=&v"(xa[3]), "=&v"(xa[4]), "=&v"(xa[5]), "=&v"(xa[6]), "=&v"(xa[7]), "=&v"(xa[8])
        : "v"(vxa), "s"(x) : "memory");
      asm volatile("s_waitcnt vmcnt(9)" : "+v"(e), "+v"(xs0), "+v"(xs1), "+v"(xs2) :: "memory");
      tag = __builtin_amdgcn_readfirstlane(e) + 1u;
      if (wg == 0 && hw == 0) { const unsigned now = lds_wait(L, C_ORG, 1); __builtin_amdgcn_raw_buffer_store_b128(u4{now, tag, now, tag}, wsr, WS_T0 + 256 * (lane >> 2) + 16 * (lane & 3), 0, 16); __builtin_amdgcn_raw_buffer_store_b128(u4{now, tag, now, tag}, wsr, WS_T0 + 4096 + 256 * (lane >> 2) + 16 * (lane & 3), 0, 16); }
      float v[24];
#pragma unroll
      for (int k = 0; k < 4; ++k) { v[2 * k] = bflo(xs0[k]); v[2 * k + 1] = bfhi(xs0[k]); v[8 + 2 * k] = bflo(xs1[k]); v[9 + 2 * k] = bfhi(xs1[k]); v[16 + 2 * k] = bflo(xs2[k]); v[17 + 2 * k] = bfhi(xs2[k]); }
      const bool t3 = (lane & 31) < 8;
      float s = 0.f, s2 = 0.f, s3 = 0.f;
#pragma unroll
      for (int k = 0; k < 8; ++k) { s += v[k]; s2 += v[8 + k]; s3 += v[16 + k]; }
      const float mean = sum_half(s + s2 + (t3 ? s3 : 0.f)) * (1.f / DM);
      s = 0.f; s2 = 0.f; s3 = 0.f;
#pragma unroll
      for (int k = 0; k < 8; ++k) { const float d1 = v[k] - mean, d2 = v[8 + k] - mean, d3 = v[16 + k] - mean; s = fmaf(d1, d1, s); s2 = fmaf(d2, d2, s2); s3 = fmaf(d3, d3, s3); }
      const float rstd = __builtin_amdgcn_rsqf(sum_half(s + s2 + (t3 ? s3 : 0.f)) * (1.f / DM) + EPS);
      float mean0 = rdl(mean, 0), mean1 = rdl(mean, 32), rstd0 = rdl(rstd, 0), rstd1 = rdl(rstd, 32);
      asm volatile("" : "+s"(mean0), "+s"(mean1), "+s"(rstd0), "+s"(rstd1));
      asm volatile("s_waitcnt vmcnt(0)" : "+v"(xa[0]), "+v"(xa[1]), "+v"(xa[2]), "+v"(xa[3]), "+v"(xa[4]), "+v"(xa[5]), "+v"(xa[6]), "+v"(xa[7]), "+v"(xa[8]) :: "memory");
#pragma unroll
      for (int k = 0; k < 9; ++k) { xa[k].x = (c & 2) ? 0x3f803f80u : xa[k].x; xa[k].y = (c & 2) ? 0x3f803f80u : xa[k].y; asm volatile("" : "+v"(xa[k])); }
      lds_set(L, C_TAG, tag);
      lds_wait<0>(L, C_WQ, 4);
      u2 fr[9];
#pragma unroll
      for (int t = 0; t < 9; ++t) fr[t] = __builtin_bit_cast(u2, __builtin_amdgcn_ds_read_tr16_b64_v4bf16((LAS bfx4*)(tra + STG + 2048 * t)));
      lds_arrive(L, C_QR);
      f4 d = {0.f, 0.f, 0.f, 0.f};
#pragma unroll
      for (int t = 0; t < 4; ++t)
        d = __builtin_amdgcn_mfma_f32_16x16x32_bf16(__builtin_bit_cast(bfx8, u4{xa[2 * t].x, xa[2 * t].y, xa[2 * t + 1].x, xa[2 * t + 1].y}), __builtin_bit_cast(bfx8, u4{fr[2 * t].x, fr[2 * t].y, fr[2 * t + 1].x, fr[2 * t + 1].y}), d, 0, 0, 0);
      d += __builtin_amdgcn_mfma_f32_16x16x16bf16_1k(__builtin_bit_cast(s4, xa[8]), __builtin_bit_cast(s4, fr[8]), f4{0.f, 0.f, 0.f, 0.f}, 0, 0, 0);
      const float v0 = rstd0 * (d[0] - mean0 * d[2]), v1 = rstd1 * (d[1] - mean1 * d[2]);
      const float n0 = dpp<X1>(v0), n1 = dpp<X1>(v1);
      const unsigned P = (c & 1) ? pk2(n1, v1) : pk2(v0, n0);
      const unsigned Q = __float_as_uint(dpp<X2>(__uint_as_float(P)));
      {
        const int fr = 16 * (lane & 3) + 4 * ((lane >> 2) & 1);
        const unsigned P2 = __builtin_amdgcn_ds_bpermute(fr, P), Q2 = __builtin_amdgcn_ds_bpermute(fr, Q);
        if (lane < 8) __builtin_amdgcn_raw_buffer_store_b128(u4{P2, tag, Q2, tag}, wsr, WS_QP + (((lane >> 2) * 16 + (cq >> 2)) * 4 + ks) * 1024 + (cq & 3) * 256 + 64 * hw + 16 * (lane & 3), 0, 16);
      }
      {
        const unsigned tq = lds_wait(L, C_ORG, 1) + DQ;
        UNTIL(tq, 0);
        const u4 r = gather1(ws + WS_QP + grp * 4096, g * 1024 + 256 * hw + 16 * c, tag);
        const float qa = add32(add16(bflo(r.x))), qb = add32(add16(bfhi(r.x))), qc = add32(add16(bflo(r.z))), qd = add32(add16(bfhi(r.z)));
        if (lane < 16) *(LAS u2*)(L + QS + 128 * hw + 8 * lane) = u2{pk2(qa, qb), pk2(qc, qd)};
        lds_arrive(L, C_Q);
      }
    }
    __builtin_amdgcn_s_setprio(0);
    {
      lds_wait<32>(L, C_LAND, MXW + 2 * hw);
      const int t = tid - 256;
      const u4 a0 = *(const GAS u4*)(w1 + 32 * t), a1 = *(const GAS u4*)(w1 + 32 * t + 16), e0 = *(const GAS u4*)(b1 + 32 * t), e1 = *(const GAS u4*)(b1 + 32 * t + 16);
      const u4 c0 = *(const GAS u4*)(w2 + 32 * t), c1 = *(const GAS u4*)(w2 + 32 * t + 16);
      const unsigned a2 = *(const GAS unsigned*)(w1 + 8192 + 4 * t), e2 = *(const GAS unsigned*)(b1 + 8192 + 4 * t), c2 = *(const GAS unsigned*)(w2 + 8192 + 4 * t);
      f2 n1[9], n0[9], t0[9], t1[9], t2[9], t3[9];
#pragma unroll
      for (int k = 0; k < 9; ++k) {
        const unsigned pa = k < 4 ? a0[k & 3] : k < 8 ? a1[k & 3] : a2, pe = k < 4 ? e0[k & 3] : k < 8 ? e1[k & 3] : e2, pc = k < 4 ? c0[k & 3] : k < 8 ? c1[k & 3] : c2;
        const f2 w = {bflo(pa), bfhi(pa)}, e = {bflo(pe), bfhi(pe)}, o = {bflo(pc), bfhi(pc)}, ee = e * e, gw = G3 * w;
        n1[k] = w * o; n0[k] = e * o;
        t0[k] = e * (ee * G3 + G1); t1[k] = w * (ee * (3.f * G3) + G1); t2[k] = 3.f * gw * w * e; t3[k] = gw * w * w;
      }
      const unsigned z0 = ((wg >> 7) << 15) + ZL + (wg & 127) * 32;
#pragma nounroll
      for (int i = 0; i < 8; ++i) {
        f2 z[4], a[4];
#pragma unroll
        for (int p = 0; p < 4; ++p) { const float zp = __uint_as_float((z0 + 4 * i + p) << 16); z[p] = f2{zp, zp}; a[p] = f2{0.f, 0.f}; }
#pragma unroll
        for (int k = 0; k < 9; ++k) {
          f2 tt[4];
#define ROW(e) _Pragma("unroll") for (int p = 0; p < 4; ++p) { e; } __builtin_amdgcn_sched_barrier(0)
          ROW(tt[p] = __builtin_elementwise_fma(t3[k], z[p], t2[k]));
          ROW(tt[p] = __builtin_elementwise_fma(tt[p], z[p], t1[k]));
          ROW(tt[p] = __builtin_elementwise_fma(tt[p], z[p], t0[k]));
          ROW(tt[p] = (f2{__builtin_amdgcn_exp2f(tt[p][0]), __builtin_amdgcn_exp2f(tt[p][1])}));
          ROW(tt[p] += (f2{1.f, 1.f}));
          ROW(tt[p] = (f2{__builtin_amdgcn_rcpf(tt[p][0]), __builtin_amdgcn_rcpf(tt[p][1])}));
          ROW(a[p] = __builtin_elementwise_fma(__builtin_elementwise_fma(n1[k], z[p], n0[k]), tt[p], a[p]); __builtin_amdgcn_sched_barrier(0));
          __builtin_amdgcn_s_sleep(10);
#undef ROW
        }
        const float acc[4] = {a[0][0] + a[0][1], a[1][0] + a[1][1], a[2][0] + a[2][1], a[3][0] + a[3][1]};
        auto s01 = __builtin_amdgcn_permlane32_swap(__float_as_uint(acc[0]), __float_as_uint(acc[1]), false, false);
        auto s23 = __builtin_amdgcn_permlane32_swap(__float_as_uint(acc[2]), __float_as_uint(acc[3]), false, false);
        const float r01 = __uint_as_float(s01[0]) + __uint_as_float(s01[1]), r23 = __uint_as_float(s23[0]) + __uint_as_float(s23[1]);
        auto sq = __builtin_amdgcn_permlane16_swap(__float_as_uint(r01), __float_as_uint(r23), false, false);
        const float r = sum_row(__uint_as_float(sq[0]) + __uint_as_float(sq[1]));
        if (c == 0) *(LAS float*)(L + HS + 128 * hw + 16 * i + 4 * ((g >> 1) + 2 * (g & 1))) = r;
      }
      if (lds_arrive_rtn(L, C_T) == 3) {
        const float bias = bflo(b2[0]);
        f4 d[4];
#pragma unroll
        for (int w = 0; w < 4; ++w) d[w] = *(LAS f4*)(L + HS + 128 * w + 16 * (lane & 7));
        const f4 y = (d[0] + d[1]) + (d[2] + d[3]);
        if (lane < 8) __builtin_amdgcn_raw_buffer_store_b128(u4{pk2(rbf(y[0]) + bias, rbf(y[1]) + bias), tag, pk2(rbf(y[2]) + bias, rbf(y[3]) + bias), tag}, wsr, WS_TB + 128 * wg + 16 * lane, 0, 16);
      }
      if (hw == 0 && proj) { lds_wait<32>(L, C_LAND, TFW); gather1(ws + WS_TB + 1024 * kvp, (c >> 3) * 16384 + 128 * (c & 7), tag); lds_set(L, C_TF, 1u); }
      if (fin) {
#pragma nounroll
        for (int i = 0; i < 2; ++i) {
          u4 v[4];
          lds_wait<32>(L, C_LAND, TFW + 2 * (2 * hw + i));
          gather4(v, ws + WS_TB + 8192 * hw + 4096 * i, 16 * lane, tag);
#pragma unroll
          for (int k = 0; k < 4; ++k) *(LAS u2*)(L + STG + 4096 * hw + 2048 * i + 512 * k + 8 * lane) = u2{v[k].x, v[k].z};
        }
        lds_arrive(L, C_W);
      }
    }
    if (proj) {
      lds_wait<16>(L, C_TF, 1);
      lds_wait<16>(L, C_PROG, 1);
      if (hw < 3) asm volatile("global_load_dword %0, %1, %2\n\ts_waitcnt vmcnt(0)" : "=&v"(cpf) : "v"(min(128u * (tid - 256), clen)), "s"(code) : "memory");
      const unsigned tw = lds_wait<4>(L, C_PROG, 2) + WD + (HALF - PHASE) / 16;
      const unsigned vwo = (lane >> 3) * 1152 + swz;
      const char* wob = wo + (size_t)(kvp * 256 + 64 * hw) * 1152 + 128 * cbp;
      LAS unsigned char* tb[4]; LAS unsigned char* sa = L + OS + 8 * lane; LAS unsigned char* sb = sa + 512 * hw;
#pragma unroll
      for (int nb = 0; nb < 4; ++nb) { tb[nb] = L + hw * RING + (4 * g + q2) * 128 + 16 * ((2 * nb + (p >> 1)) ^ (2 * ((2 * g + (q2 >> 1)) & 3))) + 8 * (p & 1); asm volatile("" : "+v"(tb[nb])); }
      unsigned ra = 18432 * (lane >> 5) + 4 * (lane & 31);
      asm volatile("" : "+v"(sa), "+v"(sb), "+v"(ra));
      lds_wait(L, C_F + hw, 122);
      UNTIL(tw, 0);
#pragma unroll
      for (int i = 0; i < 8; ++i) { DMA1(vwo, wob + i * 9216, lbase + hw * RING + 1024 * i, ""); if (i < 7) __builtin_amdgcn_s_sleep(2); }
      const unsigned tp = lds_wait(L, C_PUB, 1) + DP - (!(kvp & 1) ? HALF / 16 : 0);
      u4 fa[2], wf[2][4];
      {
        unsigned t0_, t1_, left = SPIN_G, voff = (lane >> 5) * 65536 + ((lane >> 2) & 7) * 256 + 16 * (lane & 3), from = 16 * c + 4 * g; u64 m; u4 v0, v1;
        const char* src = ws + WS_PD + (kvp * 2 + (hw >> 1)) * 2048 + 128 * (hw & 1);
        asm volatile("" : "+v"(voff), "+v"(from)); asm volatile("" : "+s"(src), "+s"(left));
        UNTIL(tp, 0);
#define TR2(r0, r1, r2, r3, a, o) "ds_read_b64_tr_b16 v[" #r0 ":" #r1 "], " a " offset:" #o "\n\tds_read_b64_tr_b16 v[" #r2 ":" #r3 "], " a " offset:" #o "+2048\n\t"
        asm volatile(LOOK_ISSUE(64) "s_waitcnt vmcnt(2)\n\t"
          TR2(100, 101, 102, 103, "%7", 0) TR2(104, 105, 106, 107, "%8", 0) TR2(108, 109, 110, 111, "%9", 0) TR2(112, 113, 114, 115, "%10", 0)
          TR2(116, 117, 118, 119, "%7", 4096) TR2(120, 121, 122, 123, "%8", 4096) TR2(124, 125, 126, 127, "%9", 4096) TR2(128, 129, 130, 131, "%10", 4096)
          LOOK_CHECK(64, "ds_bpermute_b32 v132, %6, v140\n\tds_bpermute_b32 v133, %6, v141\n\tds_bpermute_b32 v134, %6, v142\n\tds_bpermute_b32 v135, %6, v143\n\t"
                         "ds_bpermute_b32 v136, %6, v144\n\tds_bpermute_b32 v137, %6, v145\n\tds_bpermute_b32 v138, %6, v146\n\tds_bpermute_b32 v139, %6, v147\n\t")
          "s_waitcnt lgkmcnt(0)"
          : "=&v"(t0_), "=&v"(t1_), "=&s"(m), "+s"(left), "+v"(voff), "+s"(src), "+v"(from), "+v"(tb[0]), "+v"(tb[1]), "+v"(tb[2]), "+v"(tb[3]),
            "=&{v[100:103]}"(wf[0][0]), "=&{v[104:107]}"(wf[0][1]), "=&{v[108:111]}"(wf[0][2]), "=&{v[112:115]}"(wf[0][3]),
            "=&{v[116:119]}"(wf[1][0]), "=&{v[120:123]}"(wf[1][1]), "=&{v[124:127]}"(wf[1][2]), "=&{v[128:131]}"(wf[1][3]),
            "=&{v[132:135]}"(fa[0]), "=&{v[136:139]}"(fa[1]), "=&{v[140:143]}"(v0), "=&{v[144:147]}"(v1) :: "memory", "vcc", "scc");
#undef TR2
      }
      f4 d[4]; float t[4];
#pragma unroll
      for (int nb = 0; nb < 4; ++nb) d[nb] = __builtin_amdgcn_mfma_f32_16x16x32_bf16(__builtin_bit_cast(bfx8, fa[0]), __builtin_bit_cast(bfx8, wf[0][nb]), f4{0.f, 0.f, 0.f, 0.f}, 0, 0, 0);
      __builtin_amdgcn_sched_barrier(0);
#pragma unroll
      for (int nb = 0; nb < 4; ++nb) d[nb] = __builtin_amdgcn_mfma_f32_16x16x32_bf16(__builtin_bit_cast(bfx8, fa[1]), __builtin_bit_cast(bfx8, wf[1][nb]), d[nb], 0, 0, 0);
      __builtin_amdgcn_sched_barrier(0);
#pragma unroll
      for (int nb = 0; nb < 4; ++nb) { const f2 e = f2{d[nb][0], d[nb][1]} + f2{d[nb][2], d[nb][3]}; t[nb] = e[0] + e[1]; asm volatile("" : "+v"(t[nb])); }
      auto t01 = __builtin_amdgcn_permlane16_swap(__float_as_uint(t[0]), __float_as_uint(t[1]), false, false);
      auto t23 = __builtin_amdgcn_permlane16_swap(__float_as_uint(t[2]), __float_as_uint(t[3]), false, false);
      *(LAS f2*)sb = f2{__uint_as_float(t01[0]) + __uint_as_float(t01[1]), __uint_as_float(t23[0]) + __uint_as_float(t23[1])};
      if (lds_arrive_rtn(L, C_O) == 3) {
        const f2 r = (*(LAS f2*)sa + *(LAS f2*)(sa + 512)) + (*(LAS f2*)(sa + 1024) + *(LAS f2*)(sa + 1536));
        __builtin_amdgcn_raw_buffer_store_b32(min(pk2(r[0], r[1]), ~1u), wsr, ra, rbase, 16);
      }
    }
  }

  if (fin) {
    const int col = 64 * wave + 4 * (lane & 7) + 2 * ((lane >> 3) & 1) + (g & 1) + 32 * (g >> 1);
    unsigned xr = *(const GAS unsigned short*)(x + bo * 1152 + 2 * col);
    LAS unsigned char* ha = L + HS + 4 * tid; LAS unsigned char* hb = L + HS + 4 * lane;
    unsigned oo = 2 * (bo * DM + col);
    lds_wait(L, C_W, 4);
    asm volatile("s_waitcnt vmcnt(0)" : "+v"(xr), "+v"(ha), "+v"(hb), "+v"(oo) :: "memory");
    if (wave == 4 && !bo && lane < 4) __builtin_amdgcn_raw_buffer_store_b128(u4{tag, tag, tag, tag}, wsr, 16 * lane, 0, 16);
    const unsigned tp = lds_wait(L, C_PUB, 1) + DR;
    u4 v[2];
    { unsigned t0_, t1_, left = SPIN_G, voff = 16 * lane; u64 m;
      const char* src = ws + WS_R + (bo * 9 + wave) * 2048;
      asm volatile("" : "+v"(voff)); asm volatile("" : "+s"(src), "+s"(left));
      UNTIL(tp, 0);
      asm volatile(LOOK_ISSUE(1024) LOOK_CHECK(1024, "") : "=&v"(t0_), "=&v"(t1_), "=&s"(m), "+s"(left), "+v"(voff), "+s"(src), "=&{v[140:143]}"(v[0]), "=&{v[144:147]}"(v[1]) :: "memory", "vcc", "scc");
    }
    float r[4];
#pragma unroll
    for (int k = 0; k < 4; ++k) {
      auto t = __builtin_amdgcn_permlane32_swap(__float_as_uint(bflo(v[0][k]) + bflo(v[1][k])), __float_as_uint(bfhi(v[0][k]) + bfhi(v[1][k])), false, false);
      r[k] = __uint_as_float(t[0]) + __uint_as_float(t[1]);
    }
    auto t01 = __builtin_amdgcn_permlane16_swap(__float_as_uint(r[0]), __float_as_uint(r[1]), false, false);
    auto t23 = __builtin_amdgcn_permlane16_swap(__float_as_uint(r[2]), __float_as_uint(r[3]), false, false);
    const float ra = __uint_as_float(t01[0]) + __uint_as_float(t01[1]), rb = __uint_as_float(t23[0]) + __uint_as_float(t23[1]);
    const float rc = ra + dpp<ROR8>(ra), rd = rb + dpp<ROR8>(rb);
    const float h = rbf(bflo(xr) + rbf((lane & 8) ? rd : rc));
    *(LAS float*)ha = h;
    BARRIER();
    float hv[9];
#pragma unroll
    for (int k = 0; k < 9; ++k) hv[k] = *(LAS float*)(hb + 256 * k);
    const float piv = rdl(hv[0], 0);
    const f2 d0 = f2{hv[0], hv[1]} - piv, d1 = f2{hv[2], hv[3]} - piv, d2 = f2{hv[4], hv[5]} - piv, d3 = f2{hv[6], hv[7]} - piv;
    const float d4 = hv[8] - piv;
    const f2 da = (d0 + d1) + (d2 + d3), db = __builtin_elementwise_fma(d0, d0, d1 * d1) + __builtin_elementwise_fma(d2, d2, d3 * d3);
    const auto sw = __builtin_amdgcn_permlane32_swap(__float_as_uint((da[0] + d4) + da[1]), __float_as_uint(fmaf(d4, d4, db[0]) + db[1]), false, false);
    const float ss = sum_half(__uint_as_float(sw[0]) + __uint_as_float(sw[1])) * (1.f / DM);
    const float sa = rdl(ss, 0), sb = rdl(ss, 32);
    const float mean = piv + sa, var = fmaxf(sb - sa * sa, 0.f);
    const float rstd = __builtin_amdgcn_rsqf(var + EPS), scale = rstd * __builtin_amdgcn_rsqf(var * rstd * rstd + EPS);
    const unsigned zb = __builtin_bit_cast(unsigned short, (__bf16)((h - mean) * scale)), za = zb & 0x7fffu, zs = zb >> 15;
    unsigned y = *(LAS unsigned short*)(L + STG + zs * 2 * (ZN - ZL) + 2 * (min(max(za, ZL), ZN - 1) - ZL));
    for (u64 rest = __builtin_amdgcn_uicmp(za, ZL, 36); __builtin_expect(rest != 0, 0); rest &= rest - 1) {
      const int l = __builtin_ctzll(rest);
      const float z = bflo(__builtin_amdgcn_readlane(zb, l));
      float a = 0.f;
#pragma nounroll
      for (int i = 0; i < 36; ++i) {
        const unsigned pa = *(const GAS unsigned*)(w1 + 256 * i + 4 * lane), pe = *(const GAS unsigned*)(b1 + 256 * i + 4 * lane), pc = *(const GAS unsigned*)(w2 + 256 * i + 4 * lane);
        const float u0 = fmaf(z, bflo(pa), bflo(pe)), u1 = fmaf(z, bfhi(pa), bfhi(pe));
        a = fmaf(u0 * bflo(pc), __builtin_amdgcn_rcpf(1.f + __builtin_amdgcn_exp2f(u0 * fmaf(u0 * u0, G3, G1))), a);
        a = fmaf(u1 * bfhi(pc), __builtin_amdgcn_rcpf(1.f + __builtin_amdgcn_exp2f(u1 * fmaf(u1 * u1, G3, G1))), a);
      }
      const unsigned yl = pk2(rbf(sum_wave(a)) + bflo(b2[0]), 0.f);
      y = lane == l ? yl : y;
    }
    if (za >= ZN) y = zb;
    asm volatile("global_store_short %0, %1, %2 sc0 sc1" :: "v"(oo), "v"((unsigned)__builtin_bit_cast(unsigned short, (__bf16)(h + bflo(y)))), "s"(out) : "memory");
  }
  asm volatile(".Lfd_code_z:");
}

extern "C" void kernel_launch(void* const* d_in, const int* in_sizes, int n_in, void* d_out, int out_size, void* d_ws, size_t ws_size, hipStream_t stream) {
  (void)hipFuncSetAttribute((const void*)fused_decode_kernel, hipFuncAttributeMaxDynamicSharedMemorySize, LDS_BYTES);
  fused_decode_kernel<<<256, 640, LDS_BYTES, stream>>>((const char*)d_in[0], (const char*)d_in[1], (const char*)d_in[2], (const char*)d_in[3], (const char*)d_in[4],
      (const char*)d_in[6], (const char*)d_in[7], (const char*)d_in[8], (const unsigned short*)d_in[9], (unsigned short*)d_out, (char*)d_ws);
}
```
